```python
import torch

def _softmax(x, axis=-1):
    return torch.softmax(x, dim=axis)
def _gelu(x):
    return torch.nn.functional.gelu(x, approximate="tanh")
def _silu(x):
    return torch.nn.functional.silu(x)
def _sigmoid(x):
    return torch.sigmoid(x)
def _layernorm(x, axis=-1, eps=1e-5):
    m = x.mean(dim=axis, keepdim=True)
    v = x.var(dim=axis, keepdim=True, unbiased=False)
    return (x - m) / torch.sqrt(v + eps)
def _scatter_add(tgt, idx, src, axis=0):
    return tgt.clone().index_add_(axis, idx, src)
def _segment_sum(src, seg, n):
    out = torch.zeros((n,) + src.shape[1:], dtype=src.dtype)
    return out.index_add_(0, seg, src)
def _pad(x, pw):
    flat = tuple(v for pair in reversed(pw) for v in pair)
    return torch.nn.functional.pad(x, flat)

kernel_name = 'sequential_preLN__linear_attn_block__mixer_s_mlp__prompt'

def setup_inputs():

    _w = (lambda a: (lambda t: t.to(torch.bfloat16) if t.is_floating_point() else t)(torch.as_tensor(a)))
    def _A_setup():
        B, S, D = 1, 2048, 640
        return {
            "x": _w((lambda *s: torch.randn(*s, dtype=torch.bfloat16) * 0.05)(B, S, D)),
            "wqkv": _w((lambda *s: torch.randn(*s, dtype=torch.bfloat16) * 0.05)(D, 3 * D)),
            "wo": _w((lambda *s: torch.randn(*s, dtype=torch.bfloat16) * 0.05)(D, D)),
        }
    _A_in = _A_setup()

    def _B_setup():
        B, S, D, F = 1, 2048, 640, 1728
        return {
            "x":  _w((lambda *s: torch.randn(*s, dtype=torch.bfloat16) * 0.05)(B, S, D)),
            "w1": _w((lambda *s: torch.randn(*s, dtype=torch.bfloat16) * 3.2)(D, D) * (D ** -0.5)),
            "w2": _w((lambda *s: torch.randn(*s, dtype=torch.bfloat16) * 3.2)(D, D) * (D ** -0.5)),
            "w3": _w((lambda *s: torch.randn(*s, dtype=torch.bfloat16) * 3.2)(D, F) * (D ** -0.5)),
            "w4": _w((lambda *s: torch.randn(*s, dtype=torch.bfloat16) * 3.2)(F, D) * (F ** -0.5)),
        }
    _B_in = _B_setup()

    return {
        "A_x": _A_in["x"],
        "A_wqkv": _A_in["wqkv"],
        "A_wo": _A_in["wo"],
        "B_x": _B_in["x"],
        "B_w1": _B_in["w1"],
        "B_w2": _B_in["w2"],
        "B_w3": _B_in["w3"],
        "B_w4": _B_in["w4"],
    }

def reference(A_x, A_wqkv, A_wo, B_x, B_w1, B_w2, B_w3, B_w4):
    def _A_ref(x, wqkv, wo):
        B, S, D = x.shape
        H = 32; Dh = D // H
        qkv = (lambda x, *d: x.permute(*d))((x @ wqkv).reshape(B, S, 3, H, Dh), 2, 0, 3, 1, 4)
        q, k, v = qkv[0], qkv[1], qkv[2]
        phi = lambda t: torch.where(t > 0, t + 1, torch.exp(t)) + 1e-6
        pq, pk = phi(q), phi(k)
        kv = torch.einsum("bhsd,bhse->bhde", pk, v)
        z = torch.einsum("bhsd->bhd", pk)
        num = torch.einsum("bhqd,bhde->bhqe", pq, kv)
        den = torch.einsum("bhqd,bhd->bhq", pq, z)[..., None] + 1e-6
        o = num / den
        return (lambda x, *d: x.permute(*d))(o, 0, 2, 1, 3).reshape(B, S, D) @ wo

    def _B_ref(x, w1, w2, w3, w4):
        B, S, D = x.shape
        h = _gelu(_layernorm(x, axis=-1) @ w1)
        c = D // 4
        g0 = _pad(h[:, :, 0*c:1*c], ((0, 0), (1, 0), (0, 0)))[:, :S, :]
        g1 = _pad(h[:, :, 1*c:2*c], ((0, 0), (0, 1), (0, 0)))[:, 1:, :]
        g2 = _pad(h[:, :, 2*c:3*c], ((0, 0), (2, 0), (0, 0)))[:, :S, :]
        g3 = _pad(h[:, :, 3*c:   ], ((0, 0), (0, 2), (0, 0)))[:, 2:, :]
        hs = torch.concatenate([g0, g1, g2, g3], axis=-1) @ w2
        x1 = x + hs
        h2 = _gelu(_layernorm(x1, axis=-1) @ w3) @ w4
        return x1 + h2

    x = A_x
    h = x + _A_ref(_layernorm(x, axis=-1), A_wqkv, A_wo)
    h2 = h + _B_ref(_layernorm(h, axis=-1), B_w1, B_w2, B_w3, B_w4)
    return h2
```

```cpp
#include <hip/hip_runtime.h>
#include <hip/hip_bf16.h>
#include <stdint.h>

typedef unsigned short bf16_t;
__device__ __forceinline__ float bf2f(bf16_t v){ return __uint_as_float(((unsigned)v)<<16); }
__device__ __forceinline__ bf16_t f2bf(float f){ unsigned u=__float_as_uint(f); u = (u + 0x7FFF + ((u>>16)&1))>>16; return (bf16_t)u; }

constexpr int S=2048, D=640, H=32, DH=20, F=1728;

__global__ void ln_kernel(const bf16_t* in, bf16_t* out, int ncols){
  int row=blockIdx.x; __shared__ float red[256];
  float s=0; for(int c=threadIdx.x;c<ncols;c+=256) s+=bf2f(in[(size_t)row*ncols+c]);
  red[threadIdx.x]=s; __syncthreads();
  for(int o=128;o>0;o>>=1){ if(threadIdx.x<o) red[threadIdx.x]+=red[threadIdx.x+o]; __syncthreads(); }
  float m=red[0]/ncols; __syncthreads();
  float q=0; for(int c=threadIdx.x;c<ncols;c+=256){ float d=bf2f(in[(size_t)row*ncols+c])-m; q+=d*d; }
  red[threadIdx.x]=q; __syncthreads();
  for(int o=128;o>0;o>>=1){ if(threadIdx.x<o) red[threadIdx.x]+=red[threadIdx.x+o]; __syncthreads(); }
  float v=red[0]/ncols; float r=1.0f/sqrtf(v+1e-5f);
  for(int c=threadIdx.x;c<ncols;c+=256) out[(size_t)row*ncols+c]=f2bf((bf2f(in[(size_t)row*ncols+c])-m)*r);
}
__device__ __forceinline__ float gelu_t(float x){ float u=0.7978845608028654f*(x+0.044715f*x*x*x); return 0.5f*x*(1.0f+tanhf(u)); }
__global__ void gemm_kernel(const bf16_t* A, const bf16_t* W, bf16_t* C, int M, int N, int K, int act, const bf16_t* r1, const bf16_t* r2, int shift){
  int n=blockIdx.x*64+(threadIdx.x&63); int m=blockIdx.y*4+(threadIdx.x>>6);
  if(n>=N) return;
  float acc=0;
  for(int k=0;k<K;k++){
    int mm=m;
    if(shift){ int g=k/(K/4); int sh = g==0?1: g==1?-1: g==2?2:-2; mm=m-sh; }
    float a = (mm>=0 && mm<M)? bf2f(A[(size_t)mm*K+k]) : 0.f;
    acc+=a*bf2f(W[(size_t)k*N+n]);
  }
  if(act==1) acc=gelu_t(acc);
  if(r1) acc = bf2f(f2bf(acc))+bf2f(r1[(size_t)m*N+n]);
  if(r2) acc = bf2f(f2bf(acc))+bf2f(r2[(size_t)m*N+n]);
  C[(size_t)m*N+n]=f2bf(acc);
}
__device__ __forceinline__ float phi(float t){ return (t>0? t+1.0f : expf(t)) + 1e-6f; }
__global__ void kv_kernel(const bf16_t* qkv, float* kv){
  int h=blockIdx.x; int t=threadIdx.x; if(t>=420) return; int d=t/21, e=t%21;
  float acc=0;
  for(int s=0;s<S;s++){ float pk=phi(bf2f(qkv[(size_t)s*3*D + D + h*DH + d])); float v = e<20? bf2f(qkv[(size_t)s*3*D + 2*D + h*DH+e]) : 1.0f; acc+=pk*v; }
  kv[h*420+t]=acc;
}
__global__ void attn_kernel(const bf16_t* qkv, const float* kv, bf16_t* o){
  int s=blockIdx.x; int c=threadIdx.x; if(c>=D) return; int h=c/DH, e=c%DH;
  float num=0, den=0;
  for(int d=0;d<DH;d++){ float pq=phi(bf2f(qkv[(size_t)s*3*D + h*DH+d])); num+=pq*kv[h*420+d*21+e]; den+=pq*kv[h*420+d*21+20]; }
  o[(size_t)s*D+c]=f2bf(num/(den+1e-6f));
}

extern "C" void kernel_launch(void* const* d_in, const int* in_sizes, int n_in,
                              void* d_out, int out_size, void* d_ws, size_t ws_size,
                              hipStream_t stream) {
  const bf16_t* x=(const bf16_t*)d_in[0]; const bf16_t* wqkv=(const bf16_t*)d_in[1]; const bf16_t* wo=(const bf16_t*)d_in[2];
  const bf16_t* w1=(const bf16_t*)d_in[4]; const bf16_t* w2=(const bf16_t*)d_in[5]; const bf16_t* w3=(const bf16_t*)d_in[6]; const bf16_t* w4=(const bf16_t*)d_in[7];
  char* ws=(char*)d_ws; size_t MB=1<<20;
  bf16_t* xn=(bf16_t*)(ws); bf16_t* qkv=(bf16_t*)(ws+4*MB); float* kv=(float*)(ws+16*MB); bf16_t* o=(bf16_t*)(ws+20*MB);
  bf16_t* h=(bf16_t*)(ws+24*MB); bf16_t* y=(bf16_t*)(ws+28*MB); bf16_t* y2=(bf16_t*)(ws+32*MB); bf16_t* h1=(bf16_t*)(ws+36*MB);
  bf16_t* x1=(bf16_t*)(ws+40*MB); bf16_t* x1n=(bf16_t*)(ws+44*MB); bf16_t* h2=(bf16_t*)(ws+48*MB); bf16_t* t1=(bf16_t*)(ws+60*MB);
  bf16_t* out=(bf16_t*)d_out;
  ln_kernel<<<S,256,0,stream>>>(x,xn,D);
  gemm_kernel<<<dim3(3*D/64,S/4),256,0,stream>>>(xn,wqkv,qkv,S,3*D,D,0,nullptr,nullptr,0);
  kv_kernel<<<H,512,0,stream>>>(qkv,kv);
  attn_kernel<<<S,640,0,stream>>>(qkv,kv,o);
  gemm_kernel<<<dim3(D/64,S/4),256,0,stream>>>(o,wo,h,S,D,D,0,x,nullptr,0);
  ln_kernel<<<S,256,0,stream>>>(h,y,D);
  ln_kernel<<<S,256,0,stream>>>(y,y2,D);
  gemm_kernel<<<dim3(D/64,S/4),256,0,stream>>>(y2,w1,h1,S,D,D,1,nullptr,nullptr,0);
  gemm_kernel<<<dim3(D/64,S/4),256,0,stream>>>(h1,w2,x1,S,D,D,0,y,nullptr,1);
  ln_kernel<<<S,256,0,stream>>>(x1,x1n,D);
  gemm_kernel<<<dim3(F/64,S/4),256,0,stream>>>(x1n,w3,h2,S,F,D,1,nullptr,nullptr,0);
  gemm_kernel<<<dim3(D/64,S/4),256,0,stream>>>(h2,w4,out,S,D,F,0,x1,h,0);
}
```

```cpp
#include <hip/hip_runtime.h>
#include <stdint.h>

typedef unsigned short bf16_t;
typedef __attribute__((ext_vector_type(8))) short bf16x8;
typedef __attribute__((ext_vector_type(4))) short s16x4;
typedef __attribute__((ext_vector_type(4))) float f32x4;
typedef __attribute__((ext_vector_type(2))) float f32x2;
typedef __attribute__((ext_vector_type(4))) unsigned u32x4;
typedef __attribute__((ext_vector_type(2))) unsigned u32x2;
#define LDS_AS __attribute__((address_space(3)))
#define GLB_AS __attribute__((address_space(1)))

constexpr int S_ = 2048, D_ = 640, F_ = 1728;

constexpr size_t MB_ = 1u << 20;
constexpr size_t WS_PQ = 0 * MB_, WS_PKT = 4 * MB_, WS_VT = 8 * MB_, WS_ZP = 12 * MB_, WS_MT = 13 * MB_, WS_AP = 16 * MB_,
                 WS_W1T = 20 * MB_, WS_W2T = 21 * MB_, WS_W4T = 22 * MB_, WS_H = 28 * MB_, WS_HST = 32 * MB_, WS_HS = 36 * MB_,
                 WS_X1 = 40 * MB_, WS_X1ST = 44 * MB_, WS_H2 = 48 * MB_;

#define STM_A 0
#define STM_X 0

template <int MODE> __device__ __forceinline__ void st16(void* p, u32x4 v) {
  if (MODE == 0) *(u32x4*)p = v;
  else if (MODE == 1) __builtin_nontemporal_store(v, (u32x4*)p);
  else if (MODE == 2) asm volatile("global_store_dwordx4 %0, %1, off sc0 sc1" ::"v"(p), "v"(v) : "memory");
  else asm volatile("global_store_dwordx4 %0, %1, off sc0 sc1 nt" ::"v"(p), "v"(v) : "memory");
}
template <int MODE> __device__ __forceinline__ void st8(void* p, u32x2 v) {
  if (MODE == 0) *(u32x2*)p = v;
  else if (MODE == 1) __builtin_nontemporal_store(v, (u32x2*)p);
  else if (MODE == 2) asm volatile("global_store_dwordx2 %0, %1, off sc0 sc1" ::"v"(p), "v"(v) : "memory");
  else asm volatile("global_store_dwordx2 %0, %1, off sc0 sc1 nt" ::"v"(p), "v"(v) : "memory");
}

__device__ __forceinline__ unsigned cvtpk(float lo, float hi) {
  unsigned r; asm("v_cvt_pk_bf16_f32 %0, %1, %2" : "=v"(r) : "v"(lo), "v"(hi)); return r;
}
__device__ __forceinline__ float bflo(unsigned v) { return __uint_as_float(v << 16); }
__device__ __forceinline__ float bfhi(unsigned v) { return __uint_as_float(v & 0xffff0000u); }
__device__ __forceinline__ void glds16(const void* g, char* l) {
  __builtin_amdgcn_global_load_lds((const GLB_AS void*)g, (LDS_AS void*)l, 16, 0, 0);
}
__device__ __forceinline__ f32x4 mfma(bf16x8 a, bf16x8 b, f32x4 c) {
  return __builtin_amdgcn_mfma_f32_16x16x32_bf16(a, b, c, 0, 0, 0);
}
__device__ __forceinline__ bf16x8 ldsfrag(const char* p) { return *(const LDS_AS bf16x8*)p; }
__device__ __forceinline__ s16x4 ldstr(const char* p) {
  typedef short v4i16_t __attribute__((ext_vector_type(4)));
  return __builtin_bit_cast(s16x4, __builtin_amdgcn_ds_read_tr16_b64_v4i16((LDS_AS v4i16_t*)p));
}
__device__ __forceinline__ float xsum4(float x) {
  auto s = __builtin_amdgcn_permlane16_swap(__float_as_uint(x), __float_as_uint(x), false, false);
  float y = __uint_as_float(s[0]) + __uint_as_float(s[1]);
  auto t = __builtin_amdgcn_permlane32_swap(__float_as_uint(y), __float_as_uint(y), false, false);
  return __uint_as_float(t[0]) + __uint_as_float(t[1]);
}
__device__ __forceinline__ float gelu_f(float x) {
  const float c0 = -2.302208198f;
  const float c1 = -0.1029432397f;
  float e = __builtin_amdgcn_exp2f(x * (c0 + c1 * x * x));
  return x * __builtin_amdgcn_rcpf(1.0f + e);
}
__device__ __forceinline__ float phi_f(float t) {
  float e = __builtin_amdgcn_exp2f(t * 1.4426950408889634f);
  return (t > 0.f ? t + 1.0f : e) + 1e-6f;
}
#define WAITBAR(N) asm volatile("s_waitcnt vmcnt(" #N ") lgkmcnt(0)\n\ts_barrier" ::: "memory")
#define LBAR() asm volatile("s_waitcnt lgkmcnt(0)\n\ts_barrier" ::: "memory")
template <int N> struct IC { static constexpr int value = N; };
#define CBAR() asm volatile("" ::: "memory")

struct PT { u32x4 r[8]; };
__device__ __forceinline__ void pt_src(int u, const bf16_t* w1, const bf16_t* w2, const bf16_t* w4, char* ws,
                                       const bf16_t*& src, char*& dst, int& nks, int& ks, int& cg) {
  int up;
  if (u < 100) { src = w1; dst = ws + WS_W1T; nks = 20; up = u; }
  else if (u < 200) { src = w2; dst = ws + WS_W2T; nks = 20; up = u - 100; }
  else { src = w4; dst = ws + WS_W4T; nks = 54; up = u - 200; }
  ks = up / 5; cg = up - ks * 5;
}
__device__ __forceinline__ void pt_load(int u, int lane, const bf16_t* w1, const bf16_t* w2, const bf16_t* w4, char* ws, PT& p) {
  const bf16_t* src; char* dst; int nks, ks, cg;
  pt_src(u, w1, w2, w4, ws, src, dst, nks, ks, cg);
  int g = lane >> 4, c = lane & 15;
  const bf16_t* s = src + (size_t)(ks * 32 + g * 8) * D_ + cg * 128 + c * 8;
#pragma unroll
  for (int j = 0; j < 8; j++) p.r[j] = *(const u32x4*)(s + (size_t)j * D_);
}
__device__ __forceinline__ void pt_store(int u, int lane, const bf16_t* w1, const bf16_t* w2, const bf16_t* w4, char* ws, const PT& p) {
  const bf16_t* src; char* dst; int nks, ks, cg;
  pt_src(u, w1, w2, w4, ws, src, dst, nks, ks, cg);
  int g = lane >> 4, c = lane & 15;
  int c8 = cg * 16 + c, cb = c8 / 10, cc8 = c8 - cb * 10;
  char* base = dst + (size_t)cb * nks * 5120 + (size_t)ks * 5120 + g * 256;
  int tA, iA, tB, iB;
  if (cc8 < 8) { tA = (cc8 >> 2) << 1; tB = tA + 1; iA = (cc8 & 3) * 4; iB = iA; }
  else { tA = 4; tB = 4; iA = (cc8 - 8) * 8; iB = iA + 4; }
  char* pA = base + tA * 1024 + iA * 16;
  char* pB = base + tB * 1024 + iB * 16;
#pragma unroll
  for (int e = 0; e < 8; e++) {
    u32x4 o;
#pragma unroll
    for (int d = 0; d < 4; d++)
      o[d] = __builtin_amdgcn_perm(p.r[2 * d + 1][e >> 1], p.r[2 * d][e >> 1], (e & 1) ? 0x07060302u : 0x05040100u);
    st16<STM_X>((e < 4 ? pA : pB) + (e & 3) * 16, o);
  }
}

constexpr int G1_NS = 4;
constexpr int G1_TAB = G1_NS * 32768;
constexpr int G1_LDS = G1_TAB + 4096;

template <int MODE>
__global__ __launch_bounds__(512) void k_gemm128(const bf16_t* __restrict__ A, const bf16_t* __restrict__ W,
                                                 const bf16_t* __restrict__ w1, const bf16_t* __restrict__ w2,
                                                 const bf16_t* __restrict__ w4, char* __restrict__ ws) {
  extern __shared__ __attribute__((aligned(16))) char lds[];
  constexpr int NCB = MODE == 1 ? 15 : 14;
  constexpr int LDW = MODE == 1 ? 1920 : F_;
  const int tid = threadIdx.x, lane = tid & 63;
  const int w = __builtin_amdgcn_readfirstlane(tid >> 6);
  const int xcd = blockIdx.x & 7, idx = blockIdx.x >> 3;
  if (MODE == 1 && idx >= 2 * NCB) {
    int wv = ((xcd * 2 + (idx - 2 * NCB)) * 8 + w);
    PT p0, p1;
    pt_load(wv, lane, w1, w2, w4, ws, p0);
    pt_load(wv + 128, lane, w1, w2, w4, ws, p1);
    pt_store(wv, lane, w1, w2, w4, ws, p0);
    pt_store(wv + 128, lane, w1, w2, w4, ws, p1);
    return;
  }
  const int rt = xcd * 2 + idx / NCB, cb = idx % NCB;
  const int row0 = rt * 128, n0 = cb * 128;
  const int i = lane & 15, g = lane >> 4;
  const int wm = w >> 2, wn = w & 3;

  f32x4 sp0, sp1;
  if (MODE == 6) {
    const f32x4* st = (const f32x4*)(ws + WS_X1ST) + (size_t)(row0 + (tid >> 2)) * 8 + (tid & 3) * 2;
    sp0 = st[0]; sp1 = st[1];
  }
  const bf16_t* asrc = A + (size_t)(row0 + w * 8 + (lane >> 3)) * D_ + (((lane & 7) ^ (lane >> 3)) * 8);
  int wc = (lane & 15) ^ (((lane >> 4) | (((w >> 1) & 1) << 2)) << 1);
  if (MODE == 6 && cb == NCB - 1) wc &= 7;
  const bf16_t* wsrc = W + (size_t)(w * 4 + (lane >> 4)) * LDW + n0 + wc * 8;
  auto issue = [&](int st) {
    char* sl = lds + (st % G1_NS) * 32768 + w * 1024;
    const bf16_t* a = asrc + st * 64;
    const bf16_t* b = wsrc + (size_t)st * 64 * LDW;
    glds16(a, sl);
    glds16(a + (size_t)64 * D_, sl + 8192);
    glds16(b, sl + 16384);
    glds16(b + (size_t)32 * LDW, sl + 16384 + 8192);
    CBAR();
  };
#pragma unroll
  for (int s = 0; s < G1_NS; s++) issue(s);

  int aoff[4], woff[2];
#pragma unroll
  for (int s = 0; s < 4; s++) aoff[s] = (wm * 64 + ((s + wn) & 3) * 16 + i) * 128 + ((g ^ (i & 7)) << 4);
#pragma unroll
  for (int u = 0; u < 2; u++) {
    int nt = wn * 2 + (u ^ wm), q = i >> 2, p = i & 3;
    int f = q | ((g & 1) << 2);
    woff[u] = 16384 + (8 * g + q) * 256 + (((nt * 2 + (p >> 1)) ^ (f << 1)) << 4) + (p & 1) * 8;
  }
  f32x4 acc[4][2], rs = {0, 0, 0, 0}, gr = {0, 0, 0, 0}, cs = {0, 0, 0, 0};
#pragma unroll
  for (int s = 0; s < 4; s++) { acc[s][0] = f32x4{0, 0, 0, 0}; acc[s][1] = f32x4{0, 0, 0, 0}; }
  const bf16x8 ones = {0x3F80, 0x3F80, 0x3F80, 0x3F80, 0x3F80, 0x3F80, 0x3F80, 0x3F80};
  const bool wfirst = (MODE == 6) || (cb < 5);

  auto compute = [&](int st) {
    const char* sl = lds + (st % G1_NS) * 32768;
#pragma unroll
    for (int kk = 0; kk < 2; kk++) {
      bf16x8 af[4], wf[2];
#pragma unroll
      for (int u = 0; u < 2; u++) {
        s16x4 lo = ldstr(sl + woff[u] + kk * 8192), hi = ldstr(sl + woff[u] + kk * 8192 + 1024);
        wf[u] = __builtin_shufflevector(lo, hi, 0, 1, 2, 3, 4, 5, 6, 7);
      }
#pragma unroll
      for (int s = 0; s < 4; s++) af[s] = ldsfrag(sl + (aoff[s] ^ (kk * 64)));
      if (wfirst) {
#pragma unroll
        for (int s = 0; s < 4; s++) { acc[s][0] = mfma(wf[0], af[s], acc[s][0]); acc[s][1] = mfma(wf[1], af[s], acc[s][1]); }
        cs = mfma(wf[0], ones, cs);
      } else {
#pragma unroll
        for (int s = 0; s < 4; s++) { acc[s][0] = mfma(af[s], wf[0], acc[s][0]); acc[s][1] = mfma(af[s], wf[1], acc[s][1]); }
        cs = mfma(wf[0], ones, cs);
      }
      if (MODE == 1) { rs = mfma(ones, af[0], rs); gr = mfma(af[0], af[0], gr); }
    }
  };
  WAITBAR(12); compute(0);
#pragma unroll 1
  for (int st = 1; st < 7; st++) { WAITBAR(8); issue(st + 3); compute(st); }
  WAITBAR(8); compute(7);
  WAITBAR(4); compute(8);
  WAITBAR(0); compute(9);

  float* tmean = (float*)(lds + G1_TAB);
  float* trstd = tmean + 128;
  float* tcs = tmean + 256;
  float* tz = tmean + 384;
  if (i == 0) *(f32x4*)(tcs + (wn * 2 + wm) * 16 + 4 * g) = cs;
  if (MODE == 1) {
    if ((i >> 2) == g) {
      float sq = (i & 3) == 0 ? gr[0] : (i & 3) == 1 ? gr[1] : (i & 3) == 2 ? gr[2] : gr[3];
      float mean = rs[0] * (1.0f / D_);
      float var = fmaxf(sq * (1.0f / D_) - mean * mean, 0.f);
      int r = (wm * 4 + wn) * 16 + i;
      tmean[r] = mean; trstd[r] = __builtin_amdgcn_rsqf(var + 1e-5f);
    }
  } else {
    float s = 48.f * (sp0[0] + sp1[0]) + 32.f * (sp0[2] + sp1[2]);
    s += __shfl_xor(s, 1); s += __shfl_xor(s, 2);
    float mean = s * (1.0f / D_);
    float d0 = sp0[0] - mean, d1 = sp0[2] - mean, d2 = sp1[0] - mean, d3 = sp1[2] - mean;
    float q = sp0[1] + sp0[3] + sp1[1] + sp1[3] + 48.f * (d0 * d0 + d2 * d2) + 32.f * (d1 * d1 + d3 * d3);
    q += __shfl_xor(q, 1); q += __shfl_xor(q, 2);
    if ((tid & 3) == 0) { tmean[tid >> 2] = mean; trstd[tid >> 2] = __builtin_amdgcn_rsqf(q * (1.0f / D_) + 1e-5f); }
  }
  LBAR();

  const int nt0 = wn * 2 + wm, nt1 = wn * 2 + (1 ^ wm);
  if (wfirst) {
    f32x4 c0 = *(const f32x4*)(tcs + nt0 * 16 + 4 * g), c1 = *(const f32x4*)(tcs + nt1 * 16 + 4 * g);
    constexpr int LDO = MODE == 1 ? D_ : F_;
    bf16_t* out = (bf16_t*)(ws + (MODE == 1 ? WS_PQ : WS_H2));
    int col = n0 + ((g & 1) ? nt1 : nt0) * 16 + (g >> 1) * 8;
    bool valid = (MODE == 1) || (col < F_);
#pragma unroll
    for (int s = 0; s < 4; s++) {
      int r = wm * 64 + ((s + wn) & 3) * 16 + i;
      float mean = tmean[r], rstd = trstd[r];
      float v0[4], v1[4];
#pragma unroll
      for (int e = 0; e < 4; e++) {
        float a = rstd * (acc[s][0][e] - mean * c0[e]), b = rstd * (acc[s][1][e] - mean * c1[e]);
        v0[e] = MODE == 1 ? phi_f(a) : gelu_f(a);
        v1[e] = MODE == 1 ? phi_f(b) : gelu_f(b);
      }
      unsigned a0 = cvtpk(v0[0], v0[1]), a1 = cvtpk(v0[2], v0[3]), b0 = cvtpk(v1[0], v1[1]), b1 = cvtpk(v1[2], v1[3]);
      auto x0 = __builtin_amdgcn_permlane16_swap(a0, b0, false, false);
      auto x1 = __builtin_amdgcn_permlane16_swap(a1, b1, false, false);
      u32x4 o = {x0[0], x1[0], x0[1], x1[1]};
      if (valid) st16<STM_A>(out + (size_t)(row0 + r) * LDO + col, o);
    }
  } else {
    const bool isk = cb < 10;
    bf16_t* out = (bf16_t*)(ws + (isk ? WS_PKT : WS_VT));
    const int nbase = n0 - (isk ? 640 : 1280);
    f32x4 mean4[4], rstd4[4];
#pragma unroll
    for (int s = 0; s < 4; s++) {
      int r = wm * 64 + ((s + wn) & 3) * 16 + 4 * g;
      mean4[s] = *(const f32x4*)(tmean + r); rstd4[s] = *(const f32x4*)(trstd + r);
    }
#pragma unroll
    for (int u = 0; u < 2; u++) {
      int nl = (u ? nt1 : nt0) * 16 + i;
      float c = tcs[nl];
      unsigned pk[4][2];
      float zs = 0.f;
#pragma unroll
      for (int s = 0; s < 4; s++) {
        float v[4];
#pragma unroll
        for (int e = 0; e < 4; e++) {
          float a = rstd4[s][e] * (acc[s][u][e] - mean4[s][e] * c);
          v[e] = isk ? phi_f(a) : a;
        }
        pk[s][0] = cvtpk(v[0], v[1]); pk[s][1] = cvtpk(v[2], v[3]);
        zs += (bflo(pk[s][0]) + bfhi(pk[s][0])) + (bflo(pk[s][1]) + bfhi(pk[s][1]));
      }
#pragma unroll
      for (int sp = 0; sp < 2; sp++) {
        auto x0 = __builtin_amdgcn_permlane16_swap(pk[2 * sp][0], pk[2 * sp + 1][0], false, false);
        auto x1 = __builtin_amdgcn_permlane16_swap(pk[2 * sp][1], pk[2 * sp + 1][1], false, false);
        u32x4 o = {x0[0], x1[0], x0[1], x1[1]};
        int mt = (2 * sp + (g & 1) + wn) & 3;
        int srow = row0 + wm * 64 + mt * 16 + (g >> 1) * 8;
        st16<STM_X>(out + (size_t)(nbase + nl) * S_ + srow, o);
      }
      if (isk) {
        zs = xsum4(zs);
        if (g == 0) tz[wm * 128 + nl] = zs;
      }
    }
    if (isk) {
      LBAR();
      if (tid < 128) ((float*)(ws + WS_ZP))[rt * D_ + nbase + tid] = tz[tid] + tz[128 + tid];
    }
  }
}

constexpr int K2_LDS = 48 * 1024;
__global__ __launch_bounds__(512) void k_mid(const bf16_t* __restrict__ wo, const bf16_t* __restrict__ w1,
                                             const bf16_t* __restrict__ w2, const bf16_t* __restrict__ w4,
                                             char* __restrict__ ws) {
  extern __shared__ __attribute__((aligned(16))) char lds[];
  const int tid = threadIdx.x, lane = tid & 63;
  const int w = __builtin_amdgcn_readfirstlane(tid >> 6);
  const int xcd = blockIdx.x & 7, idx = blockIdx.x >> 3;
  const int i = lane & 15, g = lane >> 4;
  if (idx < 20) {
    const int h = xcd * 4 + idx / 5, j = idx % 5;
    const bf16_t* pkT = (const bf16_t*)(ws + WS_PKT);
    const bf16_t* vT = (const bf16_t*)(ws + WS_VT);
    unsigned wv[20];
    if (tid < 320) {
#pragma unroll
      for (int e = 0; e < 20; e++) wv[e] = *(const unsigned*)(wo + (size_t)(h * 20 + e) * D_ + tid * 2);
    }
    const int so = (i >> 2) * 32 + g * 8 + w * 256;
    const bf16_t* ap = pkT + (size_t)(h * 20 + j * 4 + (i & 3)) * S_ + so;
    const bf16_t* bp = vT + (size_t)(h * 20 + (i & 3)) * S_ + so;
    bf16x8 af[2], bfr[2][5];
#pragma unroll
    for (int c = 0; c < 2; c++) {
      af[c] = *(const bf16x8*)(ap + c * 128);
#pragma unroll
      for (int eb = 0; eb < 5; eb++) bfr[c][eb] = *(const bf16x8*)(bp + (size_t)eb * 4 * S_ + c * 128);
    }
    f32x4 acc[5];
#pragma unroll
    for (int eb = 0; eb < 5; eb++) {
      acc[eb] = mfma(af[0], bfr[0][eb], f32x4{0, 0, 0, 0});
      acc[eb] = mfma(af[1], bfr[1][eb], acc[eb]);
    }
    float* part = (float*)lds;
    if ((i >> 2) == g) {
#pragma unroll
      for (int eb = 0; eb < 5; eb++) *(f32x4*)(part + ((w * 4 + g) * 20 + eb * 4 + (i & 3)) * 4) = acc[eb];
    }
    __syncthreads();
    float* kvf = part + 32 * 80;
    if (tid < 320) {
      int v = tid >> 2, pp = tid & 3;
      float s = 0.f;
#pragma unroll
      for (int k = 0; k < 8; k++) s += part[(pp * 8 + k) * 80 + v];
      s += __shfl_xor(s, 1); s += __shfl_xor(s, 2);
      if (pp == 0) kvf[v] = s;
    }
    __syncthreads();
    if (tid < 320) {
      float m0[4] = {0, 0, 0, 0}, m1[4] = {0, 0, 0, 0};
#pragma unroll
      for (int e = 0; e < 20; e++) {
        f32x4 kv = *(const f32x4*)(kvf + e * 4);
        float a = bflo(wv[e]), b = bfhi(wv[e]);
#pragma unroll
        for (int d = 0; d < 4; d++) { m0[d] = fmaf(kv[d], a, m0[d]); m1[d] = fmaf(kv[d], b, m1[d]); }
      }
      int c = tid * 2, cb = c / 80, cc = c - cb * 80, t, ii;
      if (cc < 64) { t = ((cc >> 5) << 1) | ((cc >> 2) & 1); ii = (((cc >> 3) & 3) << 2) | (cc & 3); }
      else { t = 4; ii = cc - 64; }
      int k0 = h * 20 + j * 4, ks = k0 >> 5, gg = (k0 >> 3) & 3, half = (k0 >> 2) & 1;
      char* dst = ws + WS_MT + (size_t)cb * 102400 + (ks * 5 + t) * 1024 + (gg * 16 + ii) * 16 + half * 8;
      st8<STM_X>(dst, u32x2{cvtpk(m0[0], m0[1]), cvtpk(m0[2], m0[3])});
      st8<STM_X>(dst + 16, u32x2{cvtpk(m1[0], m1[1]), cvtpk(m1[2], m1[3])});
    }
  } else if (idx < 28) {
    const int r0 = xcd * 256 + (idx - 20) * 32;
    const char* src = ws + WS_PQ + (size_t)r0 * 1280;
    float* z = (float*)(lds + 40960);
    if (w >= 3) {
#pragma unroll
      for (int k = 0; k < 8; k++) { int q = (w - 3) * 8 + k; glds16(src + q * 1024 + lane * 16, lds + q * 1024); }
    }
    if (tid < 160) {
      const f32x4* zp = (const f32x4*)(ws + WS_ZP) + tid;
      f32x4 s = {0, 0, 0, 0};
      f32x4 t[16];
#pragma unroll
      for (int k = 0; k < 16; k++) t[k] = zp[k * 160];
#pragma unroll
      for (int k = 0; k < 16; k++) s += t[k];
      *(f32x4*)(z + tid * 4) = s;
    }
    asm volatile("s_waitcnt vmcnt(0)" ::: "memory");
    __syncthreads();
    const int head = lane & 31;
    f32x4 zr[5];
#pragma unroll
    for (int k = 0; k < 5; k++) zr[k] = *(const f32x4*)(z + head * 20 + k * 4);
#pragma unroll
    for (int rep = 0; rep < 2; rep++) {
      int row = rep * 16 + w * 2 + (lane >> 5);
      char* p = lds + row * 1280 + head * 40;
      u32x2 d[5];
#pragma unroll
      for (int k = 0; k < 5; k++) d[k] = *(const u32x2*)(p + k * 8);
      float den = 1e-6f;
      float dn = 0.f;
#pragma unroll
      for (int k = 0; k < 5; k++)
        dn += bflo(d[k][0]) * zr[k][0] + bfhi(d[k][0]) * zr[k][1] + bflo(d[k][1]) * zr[k][2] + bfhi(d[k][1]) * zr[k][3];
      den += dn;
      float inv = 1.0f / den;
#pragma unroll
      for (int k = 0; k < 5; k++)
        *(u32x2*)(p + k * 8) = u32x2{cvtpk(bflo(d[k][0]) * inv, bfhi(d[k][0]) * inv), cvtpk(bflo(d[k][1]) * inv, bfhi(d[k][1]) * inv)};
    }
    __syncthreads();
    char* dst = ws + WS_AP + (size_t)r0 * 1280;
#pragma unroll
    for (int k = 0; k < 5; k++) st16<STM_A>(dst + k * 8192 + tid * 16, *(const u32x4*)(lds + k * 8192 + tid * 16));
  } else {
    int u = 256 + (xcd * 4 + (idx - 28)) * 8 + w;
    if (u < 470) {
      PT p;
      pt_load(u, lane, w1, w2, w4, ws, p);
      pt_store(u, lane, w1, w2, w4, ws, p);
    }
  }
}

constexpr int G2_NS = 8, G2_SLOT = 18432;
constexpr int G2_TAB = G2_NS * G2_SLOT;
constexpr int G2_LDS = G2_TAB + 2048;

__device__ __forceinline__ void row_stats(const f32x4& a, const f32x4& b, float& mean, float& var) {
  float s = 48.f * (a[0] + b[0]) + 32.f * (a[2] + b[2]);
  s = xsum4(s);
  mean = s * (1.0f / D_);
  float d0 = a[0] - mean, d1 = a[2] - mean, d2 = b[0] - mean, d3 = b[2] - mean;
  float q = a[1] + a[3] + b[1] + b[3] + 48.f * (d0 * d0 + d2 * d2) + 32.f * (d1 * d1 + d3 * d3);
  q = xsum4(q);
  var = q * (1.0f / D_);
}

template <int MODE>
__global__ __launch_bounds__(512) void k_gemm64(const bf16_t* __restrict__ x, char* __restrict__ ws, bf16_t* __restrict__ outp) {
  extern __shared__ __attribute__((aligned(16))) char lds[];
  constexpr int NST = MODE == 7 ? 27 : 10;
  constexpr int LDA = MODE == 7 ? F_ : D_;
  const bf16_t* A = (const bf16_t*)(ws + (MODE == 3 ? WS_AP : MODE == 4 ? WS_H : MODE == 5 ? WS_HS : WS_H2));
  const char* Wt = ws + (MODE == 3 ? WS_MT : MODE == 4 ? WS_W1T : MODE == 5 ? WS_W2T : WS_W4T);
  const int tid = threadIdx.x, lane = tid & 63;
  const int w = __builtin_amdgcn_readfirstlane(tid >> 6);
  const int xcd = blockIdx.x & 7, idx = blockIdx.x >> 3;
  const int rt = xcd * 4 + (idx >> 3), cb = idx & 7;
  const int row0 = rt * 64;
  const int i = lane & 15, g = lane >> 4;
  const int mg = w & 3, ng = w >> 2;
  const int m = row0 + mg * 16 + i;
  const int colA = cb * 80 + ng * 32 + g * 8;
  const int colB = cb * 80 + 64 + g * 4;

  u32x4 rA = {0, 0, 0, 0}, rA2 = {0, 0, 0, 0};
  u32x2 rB = {0, 0}, rB2 = {0, 0};
  f32x4 sp0 = {0, 0, 0, 0}, sp1 = {0, 0, 0, 0};
  {
    const bf16_t* r1 = MODE == 3 ? x : (const bf16_t*)(ws + WS_H);
    if (MODE == 3 || MODE == 5 || MODE == 7) {
      rA = *(const u32x4*)(r1 + (size_t)m * D_ + colA);
      if (ng == 0) rB = *(const u32x2*)(r1 + (size_t)m * D_ + colB);
    }
    if (MODE == 7) {
      const bf16_t* r2 = (const bf16_t*)(ws + WS_X1);
      rA2 = *(const u32x4*)(r2 + (size_t)m * D_ + colA);
      if (ng == 0) rB2 = *(const u32x2*)(r2 + (size_t)m * D_ + colB);
    }
    if (MODE == 4 || MODE == 5) {
      const f32x4* st = (const f32x4*)(ws + WS_HST) + (size_t)m * 8 + g * 2;
      sp0 = st[0]; sp1 = st[1];
    }
  }
  const bf16_t* asrc = A + (size_t)(row0 + w * 8 + (lane >> 3)) * LDA + (((lane & 7) ^ (lane >> 3)) * 8);
  const char* wsrc = Wt + (size_t)cb * (NST * 10240) + w * 1280 + lane * 16;
  auto issue = [&](int st, int slot) {
    char* sl = lds + slot * G2_SLOT;
    glds16(asrc + st * 64, sl + w * 1024);
    const char* b = wsrc + st * 10240;
    glds16(b, sl + 8192 + w * 1280);
    CBAR();
    if (lane < 16) glds16(b + 1024, sl + 8192 + w * 1280 + 1024);
    CBAR();
  };
#pragma unroll
  for (int s = 0; s < G2_NS; s++) issue(s, s);

  const int aoff = (mg * 16 + i) * 128 + ((g ^ (i & 7)) << 4);
  const int woff = 8192 + ng * 2048 + lane * 16;
  const int woffB = 8192 + 4096 + lane * 16;
  f32x4 acc[3], cs[3];
#pragma unroll
  for (int t = 0; t < 3; t++) { acc[t] = f32x4{0, 0, 0, 0}; cs[t] = f32x4{0, 0, 0, 0}; }
  const bf16x8 ones = {0x3F80, 0x3F80, 0x3F80, 0x3F80, 0x3F80, 0x3F80, 0x3F80, 0x3F80};

  auto mainloop = [&](auto NTc) {
    constexpr int NT = decltype(NTc)::value;
    auto compute = [&](int st, int slot) {
      const char* sl = lds + slot * G2_SLOT;
      bf16x8 af[2], wf[2][NT];
#pragma unroll
      for (int kk = 0; kk < 2; kk++) {
        af[kk] = ldsfrag(sl + (aoff ^ (kk * 64)));
        wf[kk][0] = ldsfrag(sl + woff + kk * 5120);
        wf[kk][1] = ldsfrag(sl + woff + kk * 5120 + 1024);
        if (NT == 3) wf[kk][NT - 1] = ldsfrag(sl + woffB + kk * 5120);
      }
      __builtin_amdgcn_sched_barrier(0);
#pragma unroll
      for (int kk = 0; kk < 2; kk++) {
#pragma unroll
        for (int t = 0; t < NT; t++) acc[t] = mfma(wf[kk][t], af[kk], acc[t]);
      }
      if (MODE == 4 && ((st & 1) == (mg >> 1))) {
        if (mg & 1) {
#pragma unroll
          for (int t = 0; t < NT; t++) cs[t] = mfma(wf[1][t], ones, cs[t]);
        } else {
#pragma unroll
          for (int t = 0; t < NT; t++) cs[t] = mfma(wf[0][t], ones, cs[t]);
        }
      }
    };
    WAITBAR(21); compute(0, 0);
    if (NST == 10) {
      WAITBAR(18); issue(8, 0); compute(1, 1);
      WAITBAR(18); issue(9, 1); compute(2, 2);
    } else {
      int slot = 1;
#pragma unroll 1
      for (int st = 1; st <= NST - 8; st++) {
        WAITBAR(18); issue(st + 7, (slot + 7) & 7); compute(st, slot);
        slot = (slot + 1) & 7;
      }
    }
    WAITBAR(18); compute(NST - 7, (NST - 7) & 7);
    WAITBAR(15); compute(NST - 6, (NST - 6) & 7);
    WAITBAR(12); compute(NST - 5, (NST - 5) & 7);
    WAITBAR(9); compute(NST - 4, (NST - 4) & 7);
    WAITBAR(6); compute(NST - 3, (NST - 3) & 7);
    WAITBAR(3); compute(NST - 2, (NST - 2) & 7);
    WAITBAR(0); compute(NST - 1, (NST - 1) & 7);
  };
  if (ng == 0) mainloop(IC<3>{}); else mainloop(IC<2>{});

  float v[12];
  if (MODE == 3 || MODE == 5 || MODE == 7) {
    float mean = 0.f, r1 = 1.f;
    if (MODE == 5) { float var; row_stats(sp0, sp1, mean, var); r1 = __builtin_amdgcn_rsqf(var + 1e-5f); }
#pragma unroll
    for (int e = 0; e < 4; e++) {
      float a = (e & 1) ? bfhi(rA[e >> 1]) : bflo(rA[e >> 1]);
      float b = (e & 1) ? bfhi(rA[2 + (e >> 1)]) : bflo(rA[2 + (e >> 1)]);
      float c = (e & 1) ? bfhi(rB[e >> 1]) : bflo(rB[e >> 1]);
      if (MODE == 5) { a = (a - mean) * r1; b = (b - mean) * r1; c = (c - mean) * r1; }
      if (MODE == 7) {
        a += (e & 1) ? bfhi(rA2[e >> 1]) : bflo(rA2[e >> 1]);
        b += (e & 1) ? bfhi(rA2[2 + (e >> 1)]) : bflo(rA2[2 + (e >> 1)]);
        c += (e & 1) ? bfhi(rB2[e >> 1]) : bflo(rB2[e >> 1]);
      }
      v[e] = a + acc[0][e]; v[4 + e] = b + acc[1][e]; v[8 + e] = c + acc[2][e];
    }
  } else {
    float* tcs = (float*)(lds + G2_TAB);
    if (i == 0) {
      *(f32x4*)(tcs + (mg * 5 + ng * 2) * 16 + 4 * g) = cs[0];
      *(f32x4*)(tcs + (mg * 5 + ng * 2 + 1) * 16 + 4 * g) = cs[1];
      if (ng == 0) *(f32x4*)(tcs + (mg * 5 + 4) * 16 + 4 * g) = cs[2];
    }
    float mean, var;
    row_stats(sp0, sp1, mean, var);
    float r1 = __builtin_amdgcn_rsqf(var + 1e-5f);
    float c = r1 * __builtin_amdgcn_rsqf(var * r1 * r1 + 1e-5f);
    LBAR();
    f32x4 c0 = {0, 0, 0, 0}, c1 = {0, 0, 0, 0}, c2 = {0, 0, 0, 0};
#pragma unroll
    for (int k = 0; k < 4; k++) {
      c0 += *(const f32x4*)(tcs + (k * 5 + ng * 2) * 16 + 4 * g);
      c1 += *(const f32x4*)(tcs + (k * 5 + ng * 2 + 1) * 16 + 4 * g);
      c2 += *(const f32x4*)(tcs + (k * 5 + 4) * 16 + 4 * g);
    }
#pragma unroll
    for (int e = 0; e < 4; e++) {
      v[e] = gelu_f(c * (acc[0][e] - mean * c0[e]));
      v[4 + e] = gelu_f(c * (acc[1][e] - mean * c1[e]));
      v[8 + e] = gelu_f(c * (acc[2][e] - mean * c2[e]));
    }
  }
  u32x4 oA = {cvtpk(v[0], v[1]), cvtpk(v[2], v[3]), cvtpk(v[4], v[5]), cvtpk(v[6], v[7])};
  u32x2 oB = {cvtpk(v[8], v[9]), cvtpk(v[10], v[11])};
  if (MODE == 4) {
    const int grp = cb >> 1;
    const int sh = grp == 0 ? 1 : grp == 1 ? -1 : grp == 2 ? 2 : -2;
    bf16_t* hs = (bf16_t*)(ws + WS_HS);
    int mo = m + sh;
    if (mo >= 0 && mo < S_) {
      st16<STM_A>(hs + (size_t)mo * D_ + colA, oA);
      if (ng == 0) st8<STM_A>(hs + (size_t)mo * D_ + colB, oB);
    }
    int mz = sh > 0 ? m : m;
    bool zr = sh > 0 ? (m < sh) : (m >= S_ + sh);
    if (zr) {
      st16<STM_A>(hs + (size_t)mz * D_ + colA, u32x4{0, 0, 0, 0});
      if (ng == 0) st8<STM_A>(hs + (size_t)mz * D_ + colB, u32x2{0, 0});
    }
  } else {
    bf16_t* o = MODE == 3 ? (bf16_t*)(ws + WS_H) : MODE == 5 ? (bf16_t*)(ws + WS_X1) : outp;
    if (MODE == 7) {
      st16<0>(o + (size_t)m * D_ + colA, oA);
      if (ng == 0) st8<0>(o + (size_t)m * D_ + colB, oB);
    } else {
      st16<STM_A>(o + (size_t)m * D_ + colA, oA);
      if (ng == 0) st8<STM_A>(o + (size_t)m * D_ + colB, oB);
      float r[12];
#pragma unroll
      for (int e = 0; e < 4; e++) { r[2 * e] = bflo(oA[e]); r[2 * e + 1] = bfhi(oA[e]); }
      r[8] = bflo(oB[0]); r[9] = bfhi(oB[0]); r[10] = bflo(oB[1]); r[11] = bfhi(oB[1]);
      float s = 0.f;
#pragma unroll
      for (int e = 0; e < 8; e++) s += r[e];
      if (ng == 0) s += (r[8] + r[9]) + (r[10] + r[11]);
      s = xsum4(s);
      float mp = s * (ng == 0 ? (1.0f / 48.f) : (1.0f / 32.f));
      float q = 0.f;
#pragma unroll
      for (int e = 0; e < 8; e++) { float d = r[e] - mp; q += d * d; }
      if (ng == 0) {
#pragma unroll
        for (int e = 8; e < 12; e++) { float d = r[e] - mp; q += d * d; }
      }
      q = xsum4(q);
      if (g == 0) {
        f32x2* st = (f32x2*)(ws + (MODE == 3 ? WS_HST : WS_X1ST)) + (size_t)m * 16 + cb * 2 + ng;
        *st = f32x2{mp, q};
      }
    }
  }
}

extern "C" void kernel_launch(void* const* d_in, const int* in_sizes, int n_in, void* d_out, int out_size, void* d_ws,
                              size_t ws_size, hipStream_t stream) {
  const bf16_t* x = (const bf16_t*)d_in[0];
  const bf16_t* wqkv = (const bf16_t*)d_in[1];
  const bf16_t* wo = (const bf16_t*)d_in[2];
  const bf16_t* w1 = (const bf16_t*)d_in[4];
  const bf16_t* w2 = (const bf16_t*)d_in[5];
  const bf16_t* w3 = (const bf16_t*)d_in[6];
  const bf16_t* w4 = (const bf16_t*)d_in[7];
  char* ws = (char*)d_ws;
  bf16_t* out = (bf16_t*)d_out;
  (void)hipFuncSetAttribute((const void*)k_gemm128<1>, hipFuncAttributeMaxDynamicSharedMemorySize, G1_LDS);
  (void)hipFuncSetAttribute((const void*)k_gemm128<6>, hipFuncAttributeMaxDynamicSharedMemorySize, G1_LDS);
  (void)hipFuncSetAttribute((const void*)k_gemm64<3>, hipFuncAttributeMaxDynamicSharedMemorySize, G2_LDS);
  (void)hipFuncSetAttribute((const void*)k_gemm64<4>, hipFuncAttributeMaxDynamicSharedMemorySize, G2_LDS);
  (void)hipFuncSetAttribute((const void*)k_gemm64<5>, hipFuncAttributeMaxDynamicSharedMemorySize, G2_LDS);
  (void)hipFuncSetAttribute((const void*)k_gemm64<7>, hipFuncAttributeMaxDynamicSharedMemorySize, G2_LDS);
  k_gemm128<1><<<256, 512, G1_LDS, stream>>>(x, wqkv, w1, w2, w4, ws);
  k_mid<<<256, 512, K2_LDS, stream>>>(wo, w1, w2, w4, ws);
  k_gemm64<3><<<256, 512, G2_LDS, stream>>>(x, ws, out);
  k_gemm64<4><<<256, 512, G2_LDS, stream>>>(x, ws, out);
  k_gemm64<5><<<256, 512, G2_LDS, stream>>>(x, ws, out);
  k_gemm128<6><<<224, 512, G1_LDS, stream>>>((const bf16_t*)(ws + WS_X1), w3, w1, w2, w4, ws);
  k_gemm64<7><<<256, 512, G2_LDS, stream>>>(x, ws, out);
}
```

```cpp
#include <hip/hip_runtime.h>
#include <stdint.h>

typedef unsigned short bf16_t;
typedef __attribute__((ext_vector_type(8))) short bf16x8;
typedef __attribute__((ext_vector_type(4))) short s16x4;
typedef __attribute__((ext_vector_type(4))) float f32x4;
typedef __attribute__((ext_vector_type(2))) float f32x2;
typedef __attribute__((ext_vector_type(4))) unsigned u32x4;
typedef __attribute__((ext_vector_type(2))) unsigned u32x2;
#define LDS_AS __attribute__((address_space(3)))
#define GLB_AS __attribute__((address_space(1)))

constexpr int S_ = 2048, D_ = 640, F_ = 1728;

constexpr size_t MB_ = 1u << 20;
constexpr size_t WS_PQ = 0 * MB_, WS_PKT = 4 * MB_, WS_VT = 8 * MB_, WS_ZP = 12 * MB_, WS_MT = 13 * MB_, WS_AP = 16 * MB_,
                 WS_W1T = 20 * MB_, WS_W2T = 21 * MB_, WS_W4T = 22 * MB_, WS_H = 28 * MB_, WS_HST = 32 * MB_, WS_HS = 36 * MB_,
                 WS_X1 = 40 * MB_, WS_X1ST = 44 * MB_, WS_H2 = 48 * MB_, WS_CS1 = 60 * MB_, WS_CS3 = 61 * MB_;

#define STM_A 0
#define STM_X 0

template <int MODE> __device__ __forceinline__ void st16(void* p, u32x4 v) {
  if (MODE == 0) *(u32x4*)p = v;
  else if (MODE == 1) __builtin_nontemporal_store(v, (u32x4*)p);
  else if (MODE == 2) asm volatile("global_store_dwordx4 %0, %1, off sc0 sc1" ::"v"(p), "v"(v) : "memory");
  else asm volatile("global_store_dwordx4 %0, %1, off sc0 sc1 nt" ::"v"(p), "v"(v) : "memory");
}
template <int MODE> __device__ __forceinline__ void st8(void* p, u32x2 v) {
  if (MODE == 0) *(u32x2*)p = v;
  else if (MODE == 1) __builtin_nontemporal_store(v, (u32x2*)p);
  else if (MODE == 2) asm volatile("global_store_dwordx2 %0, %1, off sc0 sc1" ::"v"(p), "v"(v) : "memory");
  else asm volatile("global_store_dwordx2 %0, %1, off sc0 sc1 nt" ::"v"(p), "v"(v) : "memory");
}

__device__ __forceinline__ unsigned cvtpk(float lo, float hi) {
  unsigned r; asm("v_cvt_pk_bf16_f32 %0, %1, %2" : "=v"(r) : "v"(lo), "v"(hi)); return r;
}
__device__ __forceinline__ float bflo(unsigned v) { return __uint_as_float(v << 16); }
__device__ __forceinline__ float bfhi(unsigned v) { return __uint_as_float(v & 0xffff0000u); }
__device__ __forceinline__ void glds16(const void* g, char* l) {
  __builtin_amdgcn_global_load_lds((const GLB_AS void*)g, (LDS_AS void*)l, 16, 0, 0);
}
__device__ __forceinline__ f32x4 mfma(bf16x8 a, bf16x8 b, f32x4 c) {
  return __builtin_amdgcn_mfma_f32_16x16x32_bf16(a, b, c, 0, 0, 0);
}
__device__ __forceinline__ bf16x8 ldsfrag(const char* p) { return *(const LDS_AS bf16x8*)p; }
__device__ __forceinline__ s16x4 ldstr(const char* p) {
  typedef short v4i16_t __attribute__((ext_vector_type(4)));
  return __builtin_bit_cast(s16x4, __builtin_amdgcn_ds_read_tr16_b64_v4i16((LDS_AS v4i16_t*)p));
}
__device__ __forceinline__ float xsum4(float x) {
  auto s = __builtin_amdgcn_permlane16_swap(__float_as_uint(x), __float_as_uint(x), false, false);
  float y = __uint_as_float(s[0]) + __uint_as_float(s[1]);
  auto t = __builtin_amdgcn_permlane32_swap(__float_as_uint(y), __float_as_uint(y), false, false);
  return __uint_as_float(t[0]) + __uint_as_float(t[1]);
}
__device__ __forceinline__ float gelu_f(float x) {
  const float c0 = -2.302208198f;
  const float c1 = -0.1029432397f;
  float e = __builtin_amdgcn_exp2f(x * (c0 + c1 * x * x));
  return x * __builtin_amdgcn_rcpf(1.0f + e);
}
__device__ __forceinline__ float phi_f(float t) {
  float e = __builtin_amdgcn_exp2f(t * 1.4426950408889634f);
  return (t > 0.f ? t + 1.0f : e) + 1e-6f;
}
#define WAITBAR(N) asm volatile("s_waitcnt vmcnt(" #N ") lgkmcnt(0)\n\ts_barrier" ::: "memory")
#define LBAR() asm volatile("s_waitcnt lgkmcnt(0)\n\ts_barrier" ::: "memory")
template <int N> struct IC { static constexpr int value = N; };
#define TS_DECL
#define TS(k)
#define TS_FLUSH(kid, base)
#define CBAR() asm volatile("" ::: "memory")
template <int N> __device__ __forceinline__ void interleave_mfma_ds() {
#pragma unroll
  for (int k = 0; k < N; k++) { __builtin_amdgcn_sched_group_barrier(0x008, 1, 0); __builtin_amdgcn_sched_group_barrier(0x100, 1, 0); }
}

struct PT { u32x4 r[8]; };
__device__ __forceinline__ void pt_src(int u, const bf16_t* w1, const bf16_t* w2, const bf16_t* w4, char* ws,
                                       const bf16_t*& src, char*& dst, int& nks, int& ks, int& cg) {
  int up;
  if (u < 100) { src = w1; dst = ws + WS_W1T; nks = 20; up = u; }
  else if (u < 200) { src = w2; dst = ws + WS_W2T; nks = 20; up = u - 100; }
  else { src = w4; dst = ws + WS_W4T; nks = 54; up = u - 200; }
  ks = up / 5; cg = up - ks * 5;
}
__device__ __forceinline__ void pt_load(int u, int lane, const bf16_t* w1, const bf16_t* w2, const bf16_t* w4, char* ws, PT& p) {
  const bf16_t* src; char* dst; int nks, ks, cg;
  pt_src(u, w1, w2, w4, ws, src, dst, nks, ks, cg);
  int g = lane >> 4, c = lane & 15;
  const bf16_t* s = src + (size_t)(ks * 32 + g * 8) * D_ + cg * 128 + c * 8;
#pragma unroll
  for (int j = 0; j < 8; j++) p.r[j] = *(const u32x4*)(s + (size_t)j * D_);
}
__device__ __forceinline__ void pt_store(int u, int lane, const bf16_t* w1, const bf16_t* w2, const bf16_t* w4, char* ws, const PT& p) {
  const bf16_t* src; char* dst; int nks, ks, cg;
  pt_src(u, w1, w2, w4, ws, src, dst, nks, ks, cg);
  int g = lane >> 4, c = lane & 15;
  int c8 = cg * 16 + c, cb = c8 / 10, cc8 = c8 - cb * 10;
  char* base = dst + (size_t)cb * nks * 5120 + (size_t)ks * 5120 + g * 256;
  int tA, iA, tB, iB;
  if (cc8 < 8) { tA = (cc8 >> 2) << 1; tB = tA + 1; iA = (cc8 & 3) * 4; iB = iA; }
  else { tA = 4; tB = 4; iA = (cc8 - 8) * 8; iB = iA + 4; }
  char* pA = base + tA * 1024 + iA * 16;
  char* pB = base + tB * 1024 + iB * 16;
#pragma unroll
  for (int e = 0; e < 8; e++) {
    u32x4 o;
#pragma unroll
    for (int d = 0; d < 4; d++)
      o[d] = __builtin_amdgcn_perm(p.r[2 * d + 1][e >> 1], p.r[2 * d][e >> 1], (e & 1) ? 0x07060302u : 0x05040100u);
    st16<STM_X>((e < 4 ? pA : pB) + (e & 3) * 16, o);
  }
}

#define SBAR() asm volatile("s_barrier" ::: "memory")
template <int N> __device__ __forceinline__ void waitvm() { asm volatile("s_waitcnt vmcnt(%0)" ::"n"(N) : "memory"); }
template <int NST, int G, int D0, int DMAX, class F>
__device__ __forceinline__ void loader_loop(F&& issue) {
  int nxt = 0;
#pragma unroll 1
  for (int b = 0; b < NST; b++) {
    int tgt = b + 1 + (D0 + b < DMAX ? D0 + b : DMAX);
    if (tgt > NST) tgt = NST;
#pragma unroll 1
    while (nxt < tgt) {
      issue(nxt);
      nxt++; }
    int al = nxt - b - 1;
    if (al >= DMAX) waitvm<DMAX * G>();
    else if (DMAX > 5 && al == 5) waitvm<(DMAX > 5 ? 5 : 0) * G>();
    else if (DMAX > 4 && al == 4) waitvm<(DMAX > 4 ? 4 : 0) * G>();
    else if (DMAX > 3 && al == 3) waitvm<(DMAX > 3 ? 3 : 0) * G>();
    else if (DMAX > 2 && al == 2) waitvm<(DMAX > 2 ? 2 : 0) * G>();
    else if (DMAX > 1 && al == 1) waitvm<(DMAX > 1 ? 1 : 0) * G>();
    else waitvm<0>();
    SBAR();
  }
}

__device__ __forceinline__ void l2_touch(const char* p, int first, int n, int lane) {
#pragma unroll 1
  for (int k = lane; k < n; k += 64) {
    unsigned v;
    asm volatile("global_load_dword %0, %1, off" : "=v"(v) : "v"(p + (size_t)(first + k) * 128) : "memory");
  }
  asm volatile("s_waitcnt vmcnt(0)" ::: "memory");
}
__device__ __forceinline__ void row_stats(const f32x4& a, const f32x4& b, float& mean, float& var) {
  float s = 48.f * (a[0] + b[0]) + 32.f * (a[2] + b[2]);
  s = xsum4(s);
  mean = s * (1.0f / D_);
  float d0 = a[0] - mean, d1 = a[2] - mean, d2 = b[0] - mean, d3 = b[2] - mean;
  float q = a[1] + a[3] + b[1] + b[3] + 48.f * (d0 * d0 + d2 * d2) + 32.f * (d1 * d1 + d3 * d3);
  q = xsum4(q);
  var = q * (1.0f / D_);
}

constexpr int G1_NS = 5;
constexpr int G1_LDS = G1_NS * 32768;

template <int MODE>
__global__ __launch_bounds__(512, 2) void k_gemm128(const bf16_t* __restrict__ A, const bf16_t* __restrict__ W,
                                                    const bf16_t* __restrict__ w1, const bf16_t* __restrict__ w2,
                                                    const bf16_t* __restrict__ w4, char* __restrict__ ws) {
  extern __shared__ __attribute__((aligned(16))) char lds[];
  constexpr int NCB = MODE == 1 ? 15 : 14;
  constexpr int LDW = MODE == 1 ? 1920 : F_;
  const int tid = threadIdx.x, lane = tid & 63;
  const int w = __builtin_amdgcn_readfirstlane(tid >> 6);
  const int xcd = blockIdx.x & 7, idx = blockIdx.x >> 3;
  TS_DECL;
  TS(0);
  if (MODE == 1 && idx >= 2 * NCB) {
    int wv = ((xcd * 2 + (idx - 2 * NCB)) * 8 + w);
    PT p0, p1;
    pt_load(wv, lane, w1, w2, w4, ws, p0);
    pt_load(wv + 128, lane, w1, w2, w4, ws, p1);
    pt_store(wv, lane, w1, w2, w4, ws, p0);
    pt_store(wv + 128, lane, w1, w2, w4, ws, p1);
    return;
  }
  const int rt = xcd * 2 + idx / NCB, cb = idx % NCB;
  const int row0 = rt * 128, n0 = cb * 128;

  if (w >= 4) {
    const int p = w - 4;
    if (p < 2) {
      const bf16_t* src = A + (size_t)(row0 + p * 64 + (lane >> 3)) * D_ + (((lane & 7) ^ (lane >> 3)) * 8);
      loader_loop<10, 8, 1, 2>([&](int st) {
        char* sl = lds + (st % G1_NS) * 32768 + p * 8192;
        const bf16_t* a = src + st * 64;
#pragma unroll
        for (int j = 0; j < 8; j++) glds16(a + (size_t)j * 8 * D_, sl + j * 1024);
        CBAR();
      });
    } else {
      const int q = p - 2;
      int wc0 = (lane & 15) ^ ((lane >> 4) << 1), wc1 = wc0 ^ 8;
      if (MODE == 6 && cb == NCB - 1) { wc0 &= 7; wc1 &= 7; }
      const bf16_t* s0 = W + (size_t)(q * 32 + (lane >> 4)) * LDW + n0 + wc0 * 8;
      const bf16_t* s1 = W + (size_t)(q * 32 + (lane >> 4)) * LDW + n0 + wc1 * 8;
      loader_loop<10, 8, 1, 2>([&](int st) {
        char* sl = lds + (st % G1_NS) * 32768 + 16384 + q * 8192;
        size_t o = (size_t)st * 64 * LDW;
#pragma unroll
        for (int j = 0; j < 8; j++) glds16((((j >> 1) & 1) ? s1 : s0) + o + (size_t)j * 4 * LDW, sl + j * 1024);
        CBAR();
      });
    }
    TS(1);
    if (w == 4) TS_FLUSH(MODE, 8);
    return;
  }

  const int i = lane & 15, g = lane >> 4;
  const int wm = w >> 1, wn = w & 1;
  int aoff[4], woff[4];
#pragma unroll
  for (int s = 0; s < 4; s++) aoff[s] = (wm * 64 + ((s + 2 * wn) & 3) * 16 + i) * 128 + ((g ^ (i & 7)) << 4);
#pragma unroll
  for (int u = 0; u < 4; u++) {
    int nt = wn * 4 + ((u + 2 * wm) & 3), q = i >> 2, p = i & 3;
    int f = q | ((g & 1) << 2);
    woff[u] = 16384 + (8 * g + q) * 256 + (((nt * 2 + (p >> 1)) ^ (f << 1)) << 4) + (p & 1) * 8;
  }
  float mean4[4], rstd4[4];
  f32x4 c4[4];
  if (MODE == 6) {
    f32x4 sp[4][2];
#pragma unroll
    for (int s = 0; s < 4; s++) {
      const f32x4* st = (const f32x4*)(ws + WS_X1ST) + (size_t)(row0 + wm * 64 + ((s + 2 * wn) & 3) * 16 + i) * 8 + g * 2;
      sp[s][0] = st[0]; sp[s][1] = st[1];
    }
#pragma unroll
    for (int u = 0; u < 4; u++) {
      int n = n0 + (wn * 4 + ((u + 2 * wm) & 3)) * 16 + 4 * g;
      c4[u] = *(const f32x4*)((const float*)(ws + WS_CS3) + (n < F_ ? n : 0));
    }
#pragma unroll
    for (int s = 0; s < 4; s++) {
      float var;
      row_stats(sp[s][0], sp[s][1], mean4[s], var);
      rstd4[s] = __builtin_amdgcn_rsqf(var + 1e-5f);
    }
  }
  f32x4 acc[4][4], rs[2], gr[2], cs[2];
#pragma unroll
  for (int s = 0; s < 4; s++)
#pragma unroll
    for (int u = 0; u < 4; u++) acc[s][u] = f32x4{0, 0, 0, 0};
#pragma unroll
  for (int s = 0; s < 2; s++) { rs[s] = f32x4{0, 0, 0, 0}; gr[s] = f32x4{0, 0, 0, 0}; cs[s] = f32x4{0, 0, 0, 0}; }
  const bf16x8 ones = {0x3F80, 0x3F80, 0x3F80, 0x3F80, 0x3F80, 0x3F80, 0x3F80, 0x3F80};
  const bool wfirst = (MODE == 6) || (cb < 5);

  struct Frag { bf16x8 a[4], w[4]; };
  auto mainloop = [&](auto WF) {
    constexpr bool wf = decltype(WF)::value;
    auto rd = [&](Frag& f, int slot, int kk) {
      const char* sl = lds + slot * 32768;
#pragma unroll
      for (int u = 0; u < 4; u++) {
        s16x4 lo = ldstr(sl + woff[u] + kk * 8192), hi = ldstr(sl + woff[u] + kk * 8192 + 1024);
        f.w[u] = __builtin_shufflevector(lo, hi, 0, 1, 2, 3, 4, 5, 6, 7);
        f.a[u] = ldsfrag(sl + (aoff[u] ^ (kk * 64)));
      }
    };
    auto mm = [&](const Frag& f) {
#pragma unroll
      for (int s = 0; s < 4; s++)
#pragma unroll
        for (int u = 0; u < 4; u++) acc[s][u] = wf ? mfma(f.w[u], f.a[s], acc[s][u]) : mfma(f.a[s], f.w[u], acc[s][u]);
      if (MODE == 1) {
#pragma unroll
        for (int s = 0; s < 2; s++) {
          cs[s] = mfma(f.w[s], ones, cs[s]);
          rs[s] = mfma(ones, f.a[s], rs[s]);
          gr[s] = mfma(f.a[s], f.a[s], gr[s]);
        }
      }
    };
    Frag f0, f1;
    TS(1);
    SBAR();
    TS(2);
    __builtin_amdgcn_s_waitcnt(0xC07F);
    rd(f0, 0, 0);
    int slot = 0;
#pragma unroll 1
    for (int st = 0; st < 9; st++) {
      rd(f1, slot, 1);
      mm(f0);
      interleave_mfma_ds<12>();
      __builtin_amdgcn_sched_barrier(0);
      SBAR();
      slot = slot == G1_NS - 1 ? 0 : slot + 1;
      rd(f0, slot, 0);
      mm(f1);
      interleave_mfma_ds<12>();
      __builtin_amdgcn_sched_barrier(0);
    }
    TS(3);
    rd(f1, slot, 1);
    mm(f0);
    mm(f1);
    TS(4);
  };
  if (wfirst) mainloop(IC<1>{}); else mainloop(IC<0>{});

  const int ctid = tid;
  float* tmean = (float*)lds;
  float* trstd = tmean + 128;
  float* tcs = tmean + 256;
  float* tz = tmean + 384;
  if (MODE == 1) {
#pragma unroll
    for (int s = 0; s < 2; s++) {
      if (i == 0) *(f32x4*)(tcs + (wn * 4 + ((s + 2 * wm) & 3)) * 16 + 4 * g) = cs[s];
      if ((i >> 2) == g) {
        float sq = (i & 3) == 0 ? gr[s][0] : (i & 3) == 1 ? gr[s][1] : (i & 3) == 2 ? gr[s][2] : gr[s][3];
        float mean = rs[s][0] * (1.0f / D_);
        float var = fmaxf(sq * (1.0f / D_) - mean * mean, 0.f);
        int r = wm * 64 + ((s + 2 * wn) & 3) * 16 + i;
        tmean[r] = mean; trstd[r] = __builtin_amdgcn_rsqf(var + 1e-5f);
      }
    }
    LBAR();
  }

  if (wfirst) {
    constexpr int LDO = MODE == 1 ? D_ : F_;
    bf16_t* out = (bf16_t*)(ws + (MODE == 1 ? WS_PQ : WS_H2));
    if (MODE == 1) {
#pragma unroll
      for (int u = 0; u < 4; u++) c4[u] = *(const f32x4*)(tcs + (wn * 4 + ((u + 2 * wm) & 3)) * 16 + 4 * g);
#pragma unroll
      for (int s = 0; s < 4; s++) { int r = wm * 64 + ((s + 2 * wn) & 3) * 16 + i; mean4[s] = tmean[r]; rstd4[s] = trstd[r]; }
    }
#pragma unroll
    for (int s = 0; s < 4; s++) {
      int r = wm * 64 + ((s + 2 * wn) & 3) * 16 + i;
      float mean = mean4[s], rstd = rstd4[s];
#pragma unroll
      for (int up = 0; up < 2; up++) {
        const int u0 = 2 * up, u1 = 2 * up + 1;
        int nt0 = wn * 4 + ((u0 + 2 * wm) & 3);
        int col = n0 + (nt0 + (g & 1)) * 16 + (g >> 1) * 8;
        float v0[4], v1[4];
#pragma unroll
        for (int e = 0; e < 4; e++) {
          float a = rstd * (acc[s][u0][e] - mean * c4[u0][e]), b = rstd * (acc[s][u1][e] - mean * c4[u1][e]);
          v0[e] = MODE == 1 ? phi_f(a) : gelu_f(a);
          v1[e] = MODE == 1 ? phi_f(b) : gelu_f(b);
        }
        unsigned a0 = cvtpk(v0[0], v0[1]), a1 = cvtpk(v0[2], v0[3]), b0 = cvtpk(v1[0], v1[1]), b1 = cvtpk(v1[2], v1[3]);
        auto x0 = __builtin_amdgcn_permlane16_swap(a0, b0, false, false);
        auto x1 = __builtin_amdgcn_permlane16_swap(a1, b1, false, false);
        u32x4 o = {x0[0], x1[0], x0[1], x1[1]};
        if (MODE == 1 || col < F_) st16<STM_A>(out + (size_t)(row0 + r) * LDO + col, o);
      }
    }
  } else {
    const bool isk = cb < 10;
    bf16_t* out = (bf16_t*)(ws + (isk ? WS_PKT : WS_VT));
    const int nbase = n0 - (isk ? 640 : 1280);
    f32x4 m4[4], r4[4];
#pragma unroll
    for (int s = 0; s < 4; s++) {
      int r = wm * 64 + ((s + 2 * wn) & 3) * 16 + 4 * g;
      m4[s] = *(const f32x4*)(tmean + r); r4[s] = *(const f32x4*)(trstd + r);
    }
#pragma unroll
    for (int u = 0; u < 4; u++) {
      int nl = (wn * 4 + ((u + 2 * wm) & 3)) * 16 + i;
      float c = tcs[nl];
      unsigned pk[4][2];
      float zs = 0.f;
#pragma unroll
      for (int s = 0; s < 4; s++) {
        float v[4];
#pragma unroll
        for (int e = 0; e < 4; e++) {
          float a = r4[s][e] * (acc[s][u][e] - m4[s][e] * c);
          v[e] = isk ? phi_f(a) : a;
        }
        pk[s][0] = cvtpk(v[0], v[1]); pk[s][1] = cvtpk(v[2], v[3]);
        zs += (bflo(pk[s][0]) + bfhi(pk[s][0])) + (bflo(pk[s][1]) + bfhi(pk[s][1]));
      }
#pragma unroll
      for (int sp = 0; sp < 2; sp++) {
        auto x0 = __builtin_amdgcn_permlane16_swap(pk[2 * sp][0], pk[2 * sp + 1][0], false, false);
        auto x1 = __builtin_amdgcn_permlane16_swap(pk[2 * sp][1], pk[2 * sp + 1][1], false, false);
        u32x4 o = {x0[0], x1[0], x0[1], x1[1]};
        int mt = (2 * sp + (g & 1) + 2 * wn) & 3;
        int srow = row0 + wm * 64 + mt * 16 + (g >> 1) * 8;
        st16<STM_X>(out + (size_t)(nbase + nl) * S_ + srow, o);
      }
      if (isk) {
        zs = xsum4(zs);
        if (g == 0) tz[wm * 128 + nl] = zs;
      }
    }
    if (isk) {
      LBAR();
      if (ctid < 128) ((float*)(ws + WS_ZP))[rt * D_ + nbase + ctid] = tz[ctid] + tz[128 + ctid];
    }
  }
  TS(5);
  if (w == 0) TS_FLUSH(MODE, 0);
}
constexpr int K2_LDS = 48 * 1024;
constexpr int K2_BLOCKS = 256 + 40;
__global__ __launch_bounds__(512) void k_mid(const bf16_t* __restrict__ wo, const bf16_t* __restrict__ w1,
                                             const bf16_t* __restrict__ w2, const bf16_t* __restrict__ w3,
                                             const bf16_t* __restrict__ w4,
                                             char* __restrict__ ws) {
  extern __shared__ __attribute__((aligned(16))) char lds[];
  const int tid = threadIdx.x, lane = tid & 63;
  const int w = __builtin_amdgcn_readfirstlane(tid >> 6);
  const int xcd = blockIdx.x & 7, idx = blockIdx.x >> 3;
  const int i = lane & 15, g = lane >> 4;
  TS_DECL;
  TS(0);
  if (blockIdx.x >= 256) {
    const int u = blockIdx.x - 256;
    if (u >= 37) return;
    const bool is3 = u < 27;
    const bf16_t* src = is3 ? w3 + u * 64 : w1 + (u - 27) * 64;
    const int ld = is3 ? F_ : D_;
    float* dst = is3 ? (float*)(ws + WS_CS3) + u * 64 : (float*)(ws + WS_CS1) + (u - 27) * 64;
    const int c = tid & 7, r = tid >> 3;
    u32x4 v[10];
#pragma unroll
    for (int k = 0; k < 10; k++) v[k] = *(const u32x4*)(src + (size_t)(k * 64 + r) * ld + c * 8);
    float s[8] = {0, 0, 0, 0, 0, 0, 0, 0};
#pragma unroll
    for (int k = 0; k < 10; k++)
#pragma unroll
      for (int e = 0; e < 4; e++) { s[2 * e] += bflo(v[k][e]); s[2 * e + 1] += bfhi(v[k][e]); }
#pragma unroll
    for (int e = 0; e < 8; e++) {
      float t = xsum4(s[e]);
      t += __shfl_xor(t, 8);
      s[e] = t;
    }
    float* part = (float*)lds;
    if (lane < 8) {
      *(f32x4*)(part + w * 64 + lane * 8) = f32x4{s[0], s[1], s[2], s[3]};
      *(f32x4*)(part + w * 64 + lane * 8 + 4) = f32x4{s[4], s[5], s[6], s[7]};
    }
    __syncthreads();
    if (tid < 64) {
      float t = 0.f;
#pragma unroll
      for (int k = 0; k < 8; k++) t += part[k * 64 + tid];
      dst[tid] = t;
    }
    return;
  }
  if (idx < 20) {
    const int h = xcd * 4 + idx / 5, j = idx % 5;
    const bf16_t* pkT = (const bf16_t*)(ws + WS_PKT);
    const bf16_t* vT = (const bf16_t*)(ws + WS_VT);
    unsigned wv[20];
    if (tid < 320) {
#pragma unroll
      for (int e = 0; e < 20; e++) wv[e] = *(const unsigned*)(wo + (size_t)(h * 20 + e) * D_ + tid * 2);
    }
    const int so = (i >> 2) * 32 + g * 8 + w * 256;
    const bf16_t* ap = pkT + (size_t)(h * 20 + j * 4 + (i & 3)) * S_ + so;
    const bf16_t* bp = vT + (size_t)(h * 20 + (i & 3)) * S_ + so;
    bf16x8 af[2], bfr[2][5];
#pragma unroll
    for (int c = 0; c < 2; c++) {
      af[c] = *(const bf16x8*)(ap + c * 128);
#pragma unroll
      for (int eb = 0; eb < 5; eb++) bfr[c][eb] = *(const bf16x8*)(bp + (size_t)eb * 4 * S_ + c * 128);
    }
    f32x4 acc[5];
#pragma unroll
    for (int eb = 0; eb < 5; eb++) {
      acc[eb] = mfma(af[0], bfr[0][eb], f32x4{0, 0, 0, 0});
      acc[eb] = mfma(af[1], bfr[1][eb], acc[eb]);
    }
    float* part = (float*)lds;
    if ((i >> 2) == g) {
#pragma unroll
      for (int eb = 0; eb < 5; eb++) *(f32x4*)(part + ((w * 4 + g) * 20 + eb * 4 + (i & 3)) * 4) = acc[eb];
    }
    __syncthreads();
    float* kvf = part + 32 * 80;
    if (tid < 320) {
      int v = tid >> 2, pp = tid & 3;
      float s = 0.f;
#pragma unroll
      for (int k = 0; k < 8; k++) s += part[(pp * 8 + k) * 80 + v];
      s += __shfl_xor(s, 1); s += __shfl_xor(s, 2);
      if (pp == 0) kvf[v] = s;
    }
    __syncthreads();
    if (tid < 320) {
      float m0[4] = {0, 0, 0, 0}, m1[4] = {0, 0, 0, 0};
#pragma unroll
      for (int e = 0; e < 20; e++) {
        f32x4 kv = *(const f32x4*)(kvf + e * 4);
        float a = bflo(wv[e]), b = bfhi(wv[e]);
#pragma unroll
        for (int d = 0; d < 4; d++) { m0[d] = fmaf(kv[d], a, m0[d]); m1[d] = fmaf(kv[d], b, m1[d]); }
      }
      int c = tid * 2, cb = c / 80, cc = c - cb * 80, t, ii;
      if (cc < 64) { t = ((cc >> 5) << 1) | ((cc >> 2) & 1); ii = (((cc >> 3) & 3) << 2) | (cc & 3); }
      else { t = 4; ii = cc - 64; }
      int k0 = h * 20 + j * 4, ks = k0 >> 5, gg = (k0 >> 3) & 3, half = (k0 >> 2) & 1;
      char* dst = ws + WS_MT + (size_t)cb * 102400 + (ks * 5 + t) * 1024 + (gg * 16 + ii) * 16 + half * 8;
      st8<STM_X>(dst, u32x2{cvtpk(m0[0], m0[1]), cvtpk(m0[2], m0[3])});
      st8<STM_X>(dst + 16, u32x2{cvtpk(m1[0], m1[1]), cvtpk(m1[2], m1[3])});
    }
  } else if (idx < 28) {
    const int r0 = xcd * 256 + (idx - 20) * 32;
    const char* src = ws + WS_PQ + (size_t)r0 * 1280;
    float* z = (float*)(lds + 40960);
    if (w >= 3) {
#pragma unroll
      for (int k = 0; k < 8; k++) { int q = (w - 3) * 8 + k; glds16(src + q * 1024 + lane * 16, lds + q * 1024); }
    }
    if (tid < 160) {
      const f32x4* zp = (const f32x4*)(ws + WS_ZP) + tid;
      f32x4 s = {0, 0, 0, 0};
      f32x4 t[16];
#pragma unroll
      for (int k = 0; k < 16; k++) t[k] = zp[k * 160];
#pragma unroll
      for (int k = 0; k < 16; k++) s += t[k];
      *(f32x4*)(z + tid * 4) = s;
    }
    asm volatile("s_waitcnt vmcnt(0)" ::: "memory");
    __syncthreads();
    const int head = lane & 31;
    f32x4 zr[5];
#pragma unroll
    for (int k = 0; k < 5; k++) zr[k] = *(const f32x4*)(z + head * 20 + k * 4);
#pragma unroll
    for (int rep = 0; rep < 2; rep++) {
      int row = rep * 16 + w * 2 + (lane >> 5);
      char* p = lds + row * 1280 + head * 40;
      u32x2 d[5];
#pragma unroll
      for (int k = 0; k < 5; k++) d[k] = *(const u32x2*)(p + k * 8);
      float den = 1e-6f;
      float dn = 0.f;
#pragma unroll
      for (int k = 0; k < 5; k++)
        dn += bflo(d[k][0]) * zr[k][0] + bfhi(d[k][0]) * zr[k][1] + bflo(d[k][1]) * zr[k][2] + bfhi(d[k][1]) * zr[k][3];
      den += dn;
      float inv = 1.0f / den;
#pragma unroll
      for (int k = 0; k < 5; k++)
        *(u32x2*)(p + k * 8) = u32x2{cvtpk(bflo(d[k][0]) * inv, bfhi(d[k][0]) * inv), cvtpk(bflo(d[k][1]) * inv, bfhi(d[k][1]) * inv)};
    }
    __syncthreads();
    char* dst = ws + WS_AP + (size_t)r0 * 1280;
#pragma unroll
    for (int k = 0; k < 5; k++) st16<STM_A>(dst + k * 8192 + tid * 16, *(const u32x4*)(lds + k * 8192 + tid * 16));
  } else {
    int u = 256 + (xcd * 4 + (idx - 28)) * 8 + w;
    if (u < 470) {
      PT p;
      pt_load(u, lane, w1, w2, w4, ws, p);
      pt_store(u, lane, w1, w2, w4, ws, p);
    }
  }
  TS(5);
  if (w == 0) TS_FLUSH(2, 0);
}

#define G2_D0 3
constexpr int G2_NS = 8, G2_SLOT = 18432;
constexpr int G2_LDS = G2_NS * G2_SLOT;

template <int MODE>
__global__ __launch_bounds__(512, 2) void k_gemm64(const bf16_t* __restrict__ x, char* __restrict__ ws, bf16_t* __restrict__ outp, const bf16_t* __restrict__ w3) {
  extern __shared__ __attribute__((aligned(16))) char lds[];
  constexpr int NST = MODE == 7 ? 27 : 10;
  constexpr int LDA = MODE == 7 ? F_ : D_;
  const bf16_t* A = (const bf16_t*)(ws + (MODE == 3 ? WS_AP : MODE == 4 ? WS_H : MODE == 5 ? WS_HS : WS_H2));
  const char* Wt = ws + (MODE == 3 ? WS_MT : MODE == 4 ? WS_W1T : MODE == 5 ? WS_W2T : WS_W4T);
  const int tid = threadIdx.x, lane = tid & 63;
  const int w = __builtin_amdgcn_readfirstlane(tid >> 6);
  const int xcd = blockIdx.x & 7, idx = blockIdx.x >> 3;
  const int rt = xcd * 4 + (idx >> 3), cb = idx & 7;
  const int row0 = rt * 64;
  TS_DECL;
  TS(0);
  if (w >= 4) {
    const int p = w - 4;
    if (p < 2) {
      const bf16_t* src = A + (size_t)(row0 + p * 32 + (lane >> 3)) * LDA + (((lane & 7) ^ (lane >> 3)) * 8);
      loader_loop<NST, 4, G2_D0, 5>([&](int st) {
        char* sl = lds + (st & 7) * G2_SLOT + p * 4096;
        const bf16_t* a = src + st * 64;
#pragma unroll
        for (int j = 0; j < 4; j++) glds16(a + (size_t)j * 8 * LDA, sl + j * 1024);
        CBAR();
      });
    } else {
      const int q = p - 2;
      const char* src = Wt + (size_t)cb * (NST * 10240) + q * 5120 + lane * 16;
      loader_loop<NST, 5, G2_D0, 5>([&](int st) {
        char* sl = lds + (st & 7) * G2_SLOT + 8192 + q * 5120;
        const char* b = src + st * 10240;
#pragma unroll
        for (int j = 0; j < 5; j++) glds16(b + j * 1024, sl + j * 1024);
        CBAR();
      });
    }
    TS(1);
    if (w == 4) TS_FLUSH(MODE, 8);
    return;
  }
  const int i = lane & 15, g = lane >> 4;
  const int mg = w & 1, ng = w >> 1;
  int m[2];
  m[0] = row0 + (2 * mg + ng) * 16 + i;
  m[1] = row0 + (2 * mg + 1 - ng) * 16 + i;
  const int colA = cb * 80 + ng * 32 + g * 8;
  const int colB = cb * 80 + 64 + g * 4;

  u32x4 rA[2], rA2[2];
  u32x2 rB = {0, 0}, rB2 = {0, 0};
  f32x4 sp[2][2], c4[3];
  float mean[2] = {0.f, 0.f}, rs1[2] = {1.f, 1.f};
  {
    const bf16_t* r1 = MODE == 3 ? x : (const bf16_t*)(ws + WS_H);
    if (MODE == 3 || MODE == 5 || MODE == 7) {
#pragma unroll
      for (int a = 0; a < 2; a++) rA[a] = *(const u32x4*)(r1 + (size_t)m[a] * D_ + colA);
      rB = *(const u32x2*)(r1 + (size_t)m[0] * D_ + colB);
    }
    if (MODE == 7) {
      const bf16_t* r2 = (const bf16_t*)(ws + WS_X1);
#pragma unroll
      for (int a = 0; a < 2; a++) rA2[a] = *(const u32x4*)(r2 + (size_t)m[a] * D_ + colA);
      rB2 = *(const u32x2*)(r2 + (size_t)m[0] * D_ + colB);
    }
    if (MODE == 4 || MODE == 5) {
#pragma unroll
      for (int a = 0; a < 2; a++) {
        const f32x4* st = (const f32x4*)(ws + WS_HST) + (size_t)m[a] * 8 + g * 2;
        sp[a][0] = st[0]; sp[a][1] = st[1];
      }
    }
    if (MODE == 4) {
      const float* c = (const float*)(ws + WS_CS1) + cb * 80;
      c4[0] = *(const f32x4*)(c + ng * 32 + g * 8);
      c4[1] = *(const f32x4*)(c + ng * 32 + g * 8 + 4);
      c4[2] = *(const f32x4*)(c + 64 + g * 4);
    }
    if (MODE == 4 || MODE == 5) {
#pragma unroll
      for (int a = 0; a < 2; a++) {
        float var;
        row_stats(sp[a][0], sp[a][1], mean[a], var);
        float r1 = __builtin_amdgcn_rsqf(var + 1e-5f);
        rs1[a] = MODE == 5 ? r1 : r1 * __builtin_amdgcn_rsqf(var * r1 * r1 + 1e-5f);
      }
    }
  }
  int aoff[2];
  aoff[0] = ((2 * mg + ng) * 16 + i) * 128 + ((g ^ (i & 7)) << 4);
  aoff[1] = ((2 * mg + 1 - ng) * 16 + i) * 128 + ((g ^ (i & 7)) << 4);
  const int woff = 8192 + ng * 2048 + lane * 16;
  const int woffB = 8192 + 4096 + lane * 16;
  f32x4 acc[2][2], accB = {0, 0, 0, 0};
#pragma unroll
  for (int a = 0; a < 2; a++) { acc[a][0] = f32x4{0, 0, 0, 0}; acc[a][1] = f32x4{0, 0, 0, 0}; }

  struct Frag { bf16x8 a[2][2], w[2][3]; };
  auto rd = [&](Frag& f, int slot) {
    const char* sl = lds + slot * G2_SLOT;
#pragma unroll
    for (int kk = 0; kk < 2; kk++) {
      f.a[kk][0] = ldsfrag(sl + (aoff[0] ^ (kk * 64)));
      f.w[kk][0] = ldsfrag(sl + woff + kk * 5120);
      f.w[kk][1] = ldsfrag(sl + woff + kk * 5120 + 1024);
      f.a[kk][1] = ldsfrag(sl + (aoff[1] ^ (kk * 64)));
      f.w[kk][2] = ldsfrag(sl + woffB + kk * 5120);
    }
  };
  auto mm = [&](const Frag& f) {
#pragma unroll
    for (int kk = 0; kk < 2; kk++) {
      acc[0][0] = mfma(f.w[kk][0], f.a[kk][0], acc[0][0]);
      acc[0][1] = mfma(f.w[kk][1], f.a[kk][0], acc[0][1]);
      acc[1][0] = mfma(f.w[kk][0], f.a[kk][1], acc[1][0]);
      acc[1][1] = mfma(f.w[kk][1], f.a[kk][1], acc[1][1]);
      accB = mfma(f.w[kk][2], f.a[kk][0], accB);
    }
  };
  {
    Frag f0, f1;
    TS(1);
    asm volatile("" :: "s"(outp));
    SBAR();
    TS(2);
    __builtin_amdgcn_s_waitcnt(0xC07F);
    rd(f0, 0);
#pragma unroll 1
    for (int st = 0; st + 2 < NST + (NST & 1); st += 2) {
      SBAR();
      rd(f1, (st + 1) & 7);
      mm(f0);
      interleave_mfma_ds<10>();
      __builtin_amdgcn_sched_barrier(0);
      SBAR();
      rd(f0, (st + 2) & 7);
      mm(f1);
      interleave_mfma_ds<10>();
      __builtin_amdgcn_sched_barrier(0);
    }
    if ((NST & 1) == 0) {
      SBAR();
      TS(3);
      rd(f1, (NST - 1) & 7);
      __builtin_amdgcn_sched_barrier(0);
      mm(f0);
      mm(f1);
    } else {
      TS(3);
      mm(f0);
    }
    TS(4);
  }

  float v[2][8], vb[4];
  if (MODE == 3 || MODE == 5 || MODE == 7) {
#pragma unroll
    for (int a = 0; a < 2; a++)
#pragma unroll
      for (int e = 0; e < 8; e++) {
        float r = (e & 1) ? bfhi(rA[a][e >> 1]) : bflo(rA[a][e >> 1]);
        if (MODE == 5) r = (r - mean[a]) * rs1[a];
        if (MODE == 7) r += (e & 1) ? bfhi(rA2[a][e >> 1]) : bflo(rA2[a][e >> 1]);
        v[a][e] = r + acc[a][e >> 2][e & 3];
      }
#pragma unroll
    for (int e = 0; e < 4; e++) {
      float r = (e & 1) ? bfhi(rB[e >> 1]) : bflo(rB[e >> 1]);
      if (MODE == 5) r = (r - mean[0]) * rs1[0];
      if (MODE == 7) r += (e & 1) ? bfhi(rB2[e >> 1]) : bflo(rB2[e >> 1]);
      vb[e] = r + accB[e];
    }
  } else {
#pragma unroll
    for (int a = 0; a < 2; a++)
#pragma unroll
      for (int e = 0; e < 8; e++) v[a][e] = gelu_f(rs1[a] * (acc[a][e >> 2][e & 3] - mean[a] * c4[e >> 2][e & 3]));
#pragma unroll
    for (int e = 0; e < 4; e++) vb[e] = gelu_f(rs1[0] * (accB[e] - mean[0] * c4[2][e]));
  }
  u32x4 oA[2];
#pragma unroll
  for (int a = 0; a < 2; a++)
    oA[a] = u32x4{cvtpk(v[a][0], v[a][1]), cvtpk(v[a][2], v[a][3]), cvtpk(v[a][4], v[a][5]), cvtpk(v[a][6], v[a][7])};
  u32x2 oB = {cvtpk(vb[0], vb[1]), cvtpk(vb[2], vb[3])};
  if (MODE == 4) {
    const int grp = cb >> 1;
    const int sh = grp == 0 ? 1 : grp == 1 ? -1 : grp == 2 ? 2 : -2;
    bf16_t* hs = (bf16_t*)(ws + WS_HS);
#pragma unroll
    for (int a = 0; a < 2; a++) {
      int mo = m[a] + sh;
      if (mo >= 0 && mo < S_) {
        st16<STM_A>(hs + (size_t)mo * D_ + colA, oA[a]);
        if (a == 0) st8<STM_A>(hs + (size_t)mo * D_ + colB, oB);
      }
      bool zr = sh > 0 ? (m[a] < sh) : (m[a] >= S_ + sh);
      if (zr) {
        st16<STM_A>(hs + (size_t)m[a] * D_ + colA, u32x4{0, 0, 0, 0});
        if (a == 0) st8<STM_A>(hs + (size_t)m[a] * D_ + colB, u32x2{0, 0});
      }
    }
  } else {
    bf16_t* o = MODE == 3 ? (bf16_t*)(ws + WS_H) : MODE == 5 ? (bf16_t*)(ws + WS_X1) : outp;
#pragma unroll
    for (int a = 0; a < 2; a++) {
      if (MODE == 7) st16<0>(o + (size_t)m[a] * D_ + colA, oA[a]); else st16<STM_A>(o + (size_t)m[a] * D_ + colA, oA[a]);
    }
    if (MODE == 7) st8<0>(o + (size_t)m[0] * D_ + colB, oB); else st8<STM_A>(o + (size_t)m[0] * D_ + colB, oB);
    if (MODE != 7) {
#pragma unroll
      for (int a = 0; a < 2; a++) {
        float r[12];
#pragma unroll
        for (int e = 0; e < 4; e++) { r[2 * e] = bflo(oA[a][e]); r[2 * e + 1] = bfhi(oA[a][e]); }
        r[8] = bflo(oB[0]); r[9] = bfhi(oB[0]); r[10] = bflo(oB[1]); r[11] = bfhi(oB[1]);
        float s = 0.f;
#pragma unroll
        for (int e = 0; e < 8; e++) s += r[e];
        if (a == 0) s += (r[8] + r[9]) + (r[10] + r[11]);
        s = xsum4(s);
        float mp = s * (a == 0 ? (1.0f / 48.f) : (1.0f / 32.f));
        float q = 0.f;
#pragma unroll
        for (int e = 0; e < 8; e++) { float d = r[e] - mp; q += d * d; }
        if (a == 0) {
#pragma unroll
          for (int e = 8; e < 12; e++) { float d = r[e] - mp; q += d * d; }
        }
        q = xsum4(q);
        if (g == 0) {
          f32x2* st = (f32x2*)(ws + (MODE == 3 ? WS_HST : WS_X1ST)) + (size_t)m[a] * 16 + cb * 2 + a;
          *st = f32x2{mp, q};
        }
      }
    }
  }
  TS(5);
  if (w == 0) TS_FLUSH(MODE, 0);
}

extern "C" void kernel_launch(void* const* d_in, const int* in_sizes, int n_in, void* d_out, int out_size, void* d_ws,
                              size_t ws_size, hipStream_t stream) {
  const bf16_t* x = (const bf16_t*)d_in[0];
  const bf16_t* wqkv = (const bf16_t*)d_in[1];
  const bf16_t* wo = (const bf16_t*)d_in[2];
  const bf16_t* w1 = (const bf16_t*)d_in[4];
  const bf16_t* w2 = (const bf16_t*)d_in[5];
  const bf16_t* w3 = (const bf16_t*)d_in[6];
  const bf16_t* w4 = (const bf16_t*)d_in[7];
  char* ws = (char*)d_ws;
  bf16_t* out = (bf16_t*)d_out;
  (void)hipFuncSetAttribute((const void*)k_gemm128<1>, hipFuncAttributeMaxDynamicSharedMemorySize, G1_LDS);
  (void)hipFuncSetAttribute((const void*)k_gemm128<6>, hipFuncAttributeMaxDynamicSharedMemorySize, G1_LDS);
  (void)hipFuncSetAttribute((const void*)k_gemm64<3>, hipFuncAttributeMaxDynamicSharedMemorySize, G2_LDS);
  (void)hipFuncSetAttribute((const void*)k_gemm64<4>, hipFuncAttributeMaxDynamicSharedMemorySize, G2_LDS);
  (void)hipFuncSetAttribute((const void*)k_gemm64<5>, hipFuncAttributeMaxDynamicSharedMemorySize, G2_LDS);
  (void)hipFuncSetAttribute((const void*)k_gemm64<7>, hipFuncAttributeMaxDynamicSharedMemorySize, G2_LDS);
  k_gemm128<1><<<256, 512, G1_LDS, stream>>>(x, wqkv, w1, w2, w4, ws);
  k_mid<<<K2_BLOCKS, 512, K2_LDS, stream>>>(wo, w1, w2, w3, w4, ws);
  k_gemm64<3><<<256, 512, G2_LDS, stream>>>(x, ws, out, w3);
  k_gemm64<4><<<256, 512, G2_LDS, stream>>>(x, ws, out, w3);
  k_gemm64<5><<<256, 512, G2_LDS, stream>>>(x, ws, out, w3);
  k_gemm128<6><<<224, 512, G1_LDS, stream>>>((const bf16_t*)(ws + WS_X1), w3, w1, w2, w4, ws);
  k_gemm64<7><<<256, 512, G2_LDS, stream>>>(x, ws, out, w3);
}
```

```cpp
#include <hip/hip_runtime.h>
#include <stdint.h>

typedef unsigned short bf16_t;
typedef __attribute__((ext_vector_type(8))) short bf16x8;
typedef __attribute__((ext_vector_type(4))) short s16x4;
typedef __attribute__((ext_vector_type(4))) float f32x4;
typedef __attribute__((ext_vector_type(2))) float f32x2;
typedef __attribute__((ext_vector_type(4))) unsigned u32x4;
typedef __attribute__((ext_vector_type(2))) unsigned u32x2;
#define LDS_AS __attribute__((address_space(3)))
#define GLB_AS __attribute__((address_space(1)))

constexpr int S_ = 2048, D_ = 640, F_ = 1728;

constexpr size_t MB_ = 1u << 20;
constexpr size_t WS_FL = 0, WS_REC = 65536, WS_CSP1 = 925696, WS_CSP3 = 976896,
                 WS_WOT = 1179648, WS_W1T = 1998848, WS_W2T = 2818048, WS_W3T = 3637248, WS_W4T = 5931008,
                 WS_PQ = 8142848, WS_O = 10764288, WS_H = 13385728,
                 WS_H1G = 16007168,
                 WS_H1 = WS_H1G + 81920, WS_X1 = 18792448, WS_H2 = 21413888;
constexpr int FL_BYTES = 49152;
constexpr int T640 = 81920, T1728 = 221184;
__device__ __forceinline__ unsigned frag_off(int q, int r) { return (unsigned)((q >> 2) * 4096 + (r >> 4) * 1024 + (q & 3) * 256 + (r & 15) * 16); }

#define STM_A 0
#define STM_X 0

template <int MODE> __device__ __forceinline__ void st16(void* p, u32x4 v) {
  if (MODE == 0) *(u32x4*)p = v;
  else if (MODE == 1) __builtin_nontemporal_store(v, (u32x4*)p);
  else if (MODE == 2) asm volatile("global_store_dwordx4 %0, %1, off sc0 sc1" ::"v"(p), "v"(v) : "memory");
  else asm volatile("global_store_dwordx4 %0, %1, off sc0 sc1 nt" ::"v"(p), "v"(v) : "memory");
}
template <int MODE> __device__ __forceinline__ void st8(void* p, u32x2 v) {
  if (MODE == 0) *(u32x2*)p = v;
  else if (MODE == 1) __builtin_nontemporal_store(v, (u32x2*)p);
  else if (MODE == 2) asm volatile("global_store_dwordx2 %0, %1, off sc0 sc1" ::"v"(p), "v"(v) : "memory");
  else asm volatile("global_store_dwordx2 %0, %1, off sc0 sc1 nt" ::"v"(p), "v"(v) : "memory");
}

__device__ __forceinline__ unsigned cvtpk(float lo, float hi) {
  unsigned r; asm("v_cvt_pk_bf16_f32 %0, %1, %2" : "=v"(r) : "v"(lo), "v"(hi)); return r;
}
__device__ __forceinline__ float bflo(unsigned v) { return __uint_as_float(v << 16); }
__device__ __forceinline__ float bfhi(unsigned v) { return __uint_as_float(v & 0xffff0000u); }
__device__ __forceinline__ void glds16(const void* g, char* l) {
  __builtin_amdgcn_global_load_lds((const GLB_AS void*)g, (LDS_AS void*)l, 16, 0, 0);
}
__device__ __forceinline__ f32x4 mfma(bf16x8 a, bf16x8 b, f32x4 c) {
  return __builtin_amdgcn_mfma_f32_16x16x32_bf16(a, b, c, 0, 0, 0);
}
__device__ __forceinline__ bf16x8 ldsfrag(const char* p) { return *(const LDS_AS bf16x8*)p; }
__device__ __forceinline__ s16x4 ldstr(const char* p) {
  typedef short v4i16_t __attribute__((ext_vector_type(4)));
  return __builtin_bit_cast(s16x4, __builtin_amdgcn_ds_read_tr16_b64_v4i16((LDS_AS v4i16_t*)p));
}
__device__ __forceinline__ float xsum4(float x) {
  auto s = __builtin_amdgcn_permlane16_swap(__float_as_uint(x), __float_as_uint(x), false, false);
  float y = __uint_as_float(s[0]) + __uint_as_float(s[1]);
  auto t = __builtin_amdgcn_permlane32_swap(__float_as_uint(y), __float_as_uint(y), false, false);
  return __uint_as_float(t[0]) + __uint_as_float(t[1]);
}
__device__ __forceinline__ float gelu_f(float x) {
  const float c0 = -2.302208198f;
  const float c1 = -0.1029432397f;
  float e = __builtin_amdgcn_exp2f(x * (c0 + c1 * x * x));
  return x * __builtin_amdgcn_rcpf(1.0f + e);
}
__device__ __forceinline__ float phi_f(float t) {
  float e = __builtin_amdgcn_exp2f(t * 1.4426950408889634f);
  return (t > 0.f ? t + 1.0f : e) + 1e-6f;
}
#define WAITBAR(N) asm volatile("s_waitcnt vmcnt(" #N ") lgkmcnt(0)\n\ts_barrier" ::: "memory")
#define LBAR() asm volatile("s_waitcnt lgkmcnt(0)\n\ts_barrier" ::: "memory")
template <int N> struct IC { static constexpr int value = N; };
#define TS_DECL
#define TS(k)
#define TS_FLUSH(kid, base)
#define CBAR() asm volatile("" ::: "memory")
template <int N> __device__ __forceinline__ void interleave_mfma_ds() {
#pragma unroll
  for (int k = 0; k < N; k++) { __builtin_amdgcn_sched_group_barrier(0x008, 1, 0); __builtin_amdgcn_sched_group_barrier(0x100, 1, 0); }
}

template <int I, int N, class F> __device__ __forceinline__ void sfor(F&& f) {
  if constexpr (I < N) { f(IC<I>{}); sfor<I + 1, N>(f); }
}
typedef __attribute__((ext_vector_type(2))) __bf16 bf16x2;
__device__ __forceinline__ float dot2(unsigned a, unsigned b, float c) {
  return __builtin_amdgcn_fdot2_f32_bf16(__builtin_bit_cast(bf16x2, a), __builtin_bit_cast(bf16x2, b), c, false);
}
__device__ __forceinline__ void glds_sc1(const char* sbase, unsigned voff, unsigned ldsoff) {
  asm volatile("s_mov_b32 m0, %2\n\tglobal_load_lds_dwordx4 %0, %1 sc1" ::"v"(voff), "s"(sbase), "s"(ldsoff) : "memory");
}
__device__ __forceinline__ void glds_pl(const char* sbase, unsigned voff, unsigned ldsoff) {
  asm volatile("s_mov_b32 m0, %2\n\tglobal_load_lds_dwordx4 %0, %1" ::"v"(voff), "s"(sbase), "s"(ldsoff) : "memory");
}
template <int N> __device__ __forceinline__ void waitvm() { asm volatile("s_waitcnt vmcnt(%0)" ::"n"(N) : "memory"); }
template <int N> __device__ __forceinline__ void waitbar() { asm volatile("s_waitcnt vmcnt(%0) lgkmcnt(0)\n\ts_barrier" ::"n"(N) : "memory"); }
#define SBAR() asm volatile("s_barrier" ::: "memory")

constexpr int PT_UNITS = 890;
constexpr unsigned WS_DUM = 28u << 20;
struct PTS { const char* src; unsigned dst, csp; int ld, ks, cg, kind; };
__device__ __forceinline__ PTS pt_src(int u, const bf16_t* wo, const bf16_t* w1, const bf16_t* w2, const bf16_t* w3, const bf16_t* w4) {
  PTS s; int up, col0;
  s.ld = D_; s.kind = 0; s.csp = WS_DUM + u * 512; s.cg = 0;
  if (u < 570) {
    int nks = 20;
    if (u < 100) { s.src = (const char*)w1; s.dst = WS_W1T; up = u; }
    else if (u < 200) { s.src = (const char*)w2; s.dst = WS_W2T; up = u - 100; }
    else if (u < 300) { s.src = (const char*)wo; s.dst = WS_WOT; up = u - 200; }
    else { s.src = (const char*)w4; s.dst = WS_W4T; nks = 54; up = u - 300; }
    s.ks = up / 5; s.cg = up - s.ks * 5;
    col0 = s.cg * 128;
    if (u < 100) s.csp = WS_CSP1 + (s.ks * D_ + col0) * 4;
    s.dst += s.ks * 5120;
    s.cg |= nks << 8;
  } else {
    up = u - 570;
    s.ks = up >> 4; const int cb = (up >> 1) & 7; s.kind = 1 + (up & 1);
    s.src = (const char*)w3; s.ld = F_;
    col0 = cb * 216 + (up & 1) * 128;
    s.csp = WS_CSP3 + (s.ks * F_ + col0) * 4;
    s.dst = WS_W3T + (cb * 20 + s.ks) * 14336 + (up & 1) * 8192;
  }
  s.src += ((size_t)s.ks * 32 * s.ld + col0) * 2;
  return s;
}
#define STM_P 16
#define STM_Q 16

constexpr int K1_LDS = 5 * 32768;
#define K1_PT 1
__global__ __launch_bounds__(512, 1) void k_qkv(const bf16_t* __restrict__ x, const bf16_t* __restrict__ wqkv, const bf16_t* __restrict__ wo,
                                                const bf16_t* __restrict__ w1, const bf16_t* __restrict__ w2,
                                                const bf16_t* __restrict__ w3, const bf16_t* __restrict__ w4, char* __restrict__ ws) {
  extern __shared__ __attribute__((aligned(16))) char lds[];
  const int tid = threadIdx.x, lane = tid & 63;
  const int w = __builtin_amdgcn_readfirstlane(tid >> 6);
  const int xcd = blockIdx.x & 7, idx = blockIdx.x >> 3;
  const int rt = (xcd >> 1) * 4 + (idx >> 3), hp = (xcd & 1) * 8 + (idx & 7);
  const int i = lane & 15, g = lane >> 4, r = w & 3;
  const __amdgpu_buffer_rsrc_t rs = __builtin_amdgcn_make_buffer_rsrc(ws, 0, 0x10000000, 0x00020000);
  TS_DECL;
  TS(0);
  const unsigned voffA = (unsigned)((rt * 128 + 16 * w + (lane >> 3)) * 1280 + (((lane & 7) ^ (lane >> 3)) << 4));
  unsigned voffW;
  bool wld;
  {
    const int c = (lane & 15) ^ ((lane >> 4) << 1) ^ ((w & 1) << 3);
    const int col = c < 5 ? 8 * c : c <= 10 ? 640 + 8 * (c - 6) : c == 11 ? 1280 + 32 : 1280 + 8 * (c - 12);
    voffW = (unsigned)((8 * w + (lane >> 4)) * 3840 + (40 * hp + col) * 2);
    wld = c != 5;
  }
  auto G = [&](auto SI) {
    constexpr int st = decltype(SI)::value, sl = (st & 3) * 32768;
    glds_pl((const char*)x + st * 128, voffA, sl + w * 2048);
    glds_pl((const char*)x + st * 128 + 8 * 1280, voffA, sl + w * 2048 + 1024);
    if (wld) {
      glds_pl((const char*)wqkv + st * 64 * 3840, voffW, sl + 16384 + w * 2048);
      glds_pl((const char*)wqkv + st * 64 * 3840 + 4 * 3840, voffW, sl + 16384 + w * 2048 + 1024);
    }
  };
  sfor<0, 4>(G);
  if (!wld) {
    const u32x4 one = {0x3F803F80u, 0x3F803F80u, 0x3F803F80u, 0x3F803F80u};
#pragma unroll
    for (int k = 0; k < 8; k++) *(LDS_AS u32x4*)(lds + (k >> 1) * 32768 + 16384 + w * 2048 + (k & 1) * 1024 + lane * 16) = one;
  }
  int aoff[2];
#pragma unroll
  for (int m = 0; m < 2; m++) aoff[m] = ((2 * r + m) * 16 + i) * 128 + ((g ^ (i & 7)) << 4);
  float* tmean = (float*)(lds + 65536);
  float* trstd = tmean + 128;
  float* tcs = tmean + 256;
  char* red = lds + 65536 + 4096;
  const bf16x8 ones = {0x3F80, 0x3F80, 0x3F80, 0x3F80, 0x3F80, 0x3F80, 0x3F80, 0x3F80};
  const f32x4 z4 = {0, 0, 0, 0};

  auto role = [&](auto ISQ_, auto HASU_) {
    constexpr bool ISQ = decltype(ISQ_)::value, HASU = decltype(HASU_)::value;
    constexpr int NT = ISQ ? 3 : 5, NT0 = ISQ ? 0 : 3, X = HASU ? 8 : 0, PS = HASU ? 10 : 0;
    PTS ps;
    if constexpr (HASU) {
      ps = pt_src(blockIdx.x * 4 + r, wo, w1, w2, w3, w4);
      int c0 = i ^ (g << 1), c1 = c0 ^ 8;
      if (ps.kind == 2) { c0 = c0 < 10 ? c0 : 10; c1 = c1 < 10 ? c1 : 10; }
      const unsigned v0 = (unsigned)(g * ps.ld * 2 + c0 * 16), v1 = (unsigned)(g * ps.ld * 2 + c1 * 16);
#pragma unroll
      for (int j = 0; j < 8; j++) glds_pl(ps.src + (size_t)j * 8 * ps.ld, ((j >> 1) & 1) ? v1 : v0, 131072 + r * 8192 + j * 1024);
    }
    int woff[NT];
#pragma unroll
    for (int u = 0; u < NT; u++) {
      const int nt = NT0 + u, q = i >> 2, p = i & 3, f = q | ((g & 1) << 2);
      woff[u] = 16384 + (8 * g + q) * 256 + (((nt * 2 + (p >> 1)) ^ (f << 1)) << 4) + (p & 1) * 8;
    }
    f32x4 acc[2][NT], cs[NT], gr[2] = {z4, z4};
#pragma unroll
    for (int u = 0; u < NT; u++) { acc[0][u] = z4; acc[1][u] = z4; cs[u] = z4; }
    struct Frag { bf16x8 a[2], w[NT]; };
    auto rd = [&](Frag& f, int slot, int kk) {
      const char* sl = lds + slot * 32768;
#pragma unroll
      for (int m = 0; m < 2; m++) f.a[m] = ldsfrag(sl + (aoff[m] ^ (kk * 64)));
#pragma unroll
      for (int u = 0; u < NT; u++) {
        s16x4 lo = ldstr(sl + woff[u] + kk * 8192), hi = ldstr(sl + woff[u] + kk * 8192 + 1024);
        f.w[u] = __builtin_shufflevector(lo, hi, 0, 1, 2, 3, 4, 5, 6, 7);
      }
    };
    auto mm = [&](const Frag& f) {
#pragma unroll
      for (int u = 0; u < NT; u++)
#pragma unroll
        for (int m = 0; m < 2; m++) acc[m][u] = ISQ ? mfma(f.w[u], f.a[m], acc[m][u]) : mfma(f.a[m], f.w[u], acc[m][u]);
      if constexpr (ISQ) { gr[0] = mfma(f.a[0], f.a[0], gr[0]); gr[1] = mfma(f.a[1], f.a[1], gr[1]); }
    };
    auto mcs = [&](const Frag& f, int sel) {
      if (r == sel) {
#pragma unroll
        for (int u = 0; u < NT; u++) cs[u] = ISQ ? mfma(f.w[u], ones, cs[u]) : mfma(ones, f.w[u], cs[u]);
      }
    };
    auto ptile = [&]() {
      const int q = i >> 2, p = i & 3, f = q | ((g & 1) << 2), pp = 2 * (p & 1) + (p >> 1);
      const char* sb = lds + 131072 + r * 8192 + (8 * g + q) * 256;
      const int nb = (ps.cg >> 8) * 5120, cg8 = (ps.cg & 255) * 8;
      float cv[8];
#pragma unroll
      for (int j = 0; j < 8; j++) {
        int jj = j; bool n32; unsigned dst;
        if (ps.kind == 0) { const int Gi = cg8 + j, cb = Gi / 5, r5 = Gi - cb * 5; n32 = r5 < 4; dst = cb * nb + (n32 ? (r5 >> 1) * 2048 + (r5 & 1) * 256 : 4096); }
        else if (ps.kind == 1) { n32 = true; dst = (j >> 1) * 2048 + (j & 1) * 256; }
        else { n32 = false; jj = j < 5 ? j : 5; dst = jj * 1024; }
        const int pe = n32 ? pp : p;
        const char* a = sb + (((jj * 2 + (pe >> 1)) ^ (f << 1)) << 4) + (pe & 1) * 8;
        s16x4 lo = ldstr(a), hi = ldstr(a + 1024);
        bf16x8 v = __builtin_shufflevector(lo, hi, 0, 1, 2, 3, 4, 5, 6, 7);
        const unsigned vo = n32 ? (unsigned)((g >> 1) * 1024 + (32 * (g & 1) + i) * 16) : (unsigned)(lane * 16);
        __builtin_amdgcn_raw_buffer_store_b128(__builtin_bit_cast(u32x4, v), rs, vo, ps.dst + dst, STM_P);
        cv[j] = mfma(ones, v, z4)[0];
      }
      const bool n32a = ps.kind == 1;
#pragma unroll
      for (int h = 0; h < 2; h++) {
        const float s = g == 0 ? cv[4 * h] : g == 1 ? cv[4 * h + 1] : g == 2 ? cv[4 * h + 2] : cv[4 * h + 3];
        int Gj = 4 * h + g;
        bool n32 = n32a;
        if (ps.kind == 0) { const int Gi = cg8 + Gj; n32 = Gi - (Gi / 5) * 5 < 4; }
        const int ci = n32 ? 8 * ((i >> 2) & 1) + 4 * (i >> 3) + (i & 3) : i;
        int col = Gj * 16 + ci;
        if (ps.kind == 2 && col >= 88) col = 87 + 0 * col;
        const bool ok = !(ps.kind == 2 && Gj * 16 + ci >= 88);
        __builtin_amdgcn_raw_buffer_store_b32(__float_as_uint(s), rs, ok ? ps.csp + (unsigned)col * 4 : WS_DUM + 890 * 512 + lane * 4, 0, STM_P);
      }
    };
    Frag f0, f1;
    waitbar<12 + X>();
    TS(1);
    rd(f0, 0, 0);
    sfor<0, 10>([&](auto SI) {
      constexpr int s = decltype(SI)::value;
      rd(f1, s & 3, 1);
      mm(f0);
      interleave_mfma_ds<2 * NT>();
      __builtin_amdgcn_sched_barrier(0);
      mcs(f0, (2 * s) & 3);
      __builtin_amdgcn_sched_barrier(0);
      if constexpr (s < 9) {
        constexpr int b = s + 1;
        constexpr int yo = b <= 3 ? 8 + X : b == 4 ? 8 : b <= 7 ? 8 + PS : b == 8 ? 4 : 0;
        waitbar<yo>();
        if constexpr (b + 3 < 10) G(IC<b + 3>{});
        rd(f0, b & 3, 0);
      }
      mm(f1);
      if constexpr (s < 9) interleave_mfma_ds<2 * NT>();
      __builtin_amdgcn_sched_barrier(0);
      mcs(f1, (2 * s + 1) & 3);
      __builtin_amdgcn_sched_barrier(0);
      if constexpr (s == 3 && HASU) {
        ptile();
        __builtin_amdgcn_sched_barrier(0);
      }
    });
    TS(2);
    if constexpr (ISQ) {
#pragma unroll
      for (int m = 0; m < 2; m++) {
        auto sw = __builtin_amdgcn_permlane32_swap(__float_as_uint(acc[m][2][0]), __float_as_uint(acc[m][2][0]), false, false);
        const float mean = __uint_as_float(sw[1]) * (1.0f / D_);
        const float q = (i & 3) == 0 ? gr[m][0] : (i & 3) == 1 ? gr[m][1] : (i & 3) == 2 ? gr[m][2] : gr[m][3];
        const float rstd = __builtin_amdgcn_rsqf(fmaxf(q * (1.0f / D_) - mean * mean, 0.f) + 1e-5f);
        if ((i >> 2) == g) { tmean[32 * r + 16 * m + i] = mean; trstd[32 * r + 16 * m + i] = rstd; }
      }
      if (i == 0) {
#pragma unroll
        for (int u = 0; u < 3; u++) *(f32x4*)(tcs + r * 48 + u * 16 + 4 * g) = cs[u];
      }
      LBAR();
      TS(3);
      f32x4 c4[3];
      float mean[2], rstd[2];
#pragma unroll
      for (int m = 0; m < 2; m++) { mean[m] = tmean[32 * r + 16 * m + i]; rstd[m] = trstd[32 * r + 16 * m + i]; }
#pragma unroll
      for (int u = 0; u < 3; u++) {
        const float* p = tcs + u * 16 + 4 * g;
        c4[u] = (*(const f32x4*)p + *(const f32x4*)(p + 48)) + (*(const f32x4*)(p + 96) + *(const f32x4*)(p + 144));
      }
      unsigned pk[2][3][2];
#pragma unroll
      for (int m = 0; m < 2; m++)
#pragma unroll
        for (int u = 0; u < 3; u++) {
          float v[4];
#pragma unroll
          for (int e = 0; e < 4; e++) v[e] = phi_f(rstd[m] * (acc[m][u][e] - mean[m] * c4[u][e]));
          pk[m][u][0] = cvtpk(v[0], v[1]); pk[m][u][1] = cvtpk(v[2], v[3]);
        }
      const unsigned qb = (unsigned)(((rt * 2 + (r >> 1)) * 8 + (hp >> 1)) * 10240 + (hp & 1) * 5120 + ((r & 1) * 32 + i) * 16);
#pragma unroll
      for (int m = 0; m < 2; m++) {
        auto x0 = __builtin_amdgcn_permlane16_swap(pk[m][0][0], pk[m][1][0], false, false);
        auto x1 = __builtin_amdgcn_permlane16_swap(pk[m][0][1], pk[m][1][1], false, false);
        __builtin_amdgcn_raw_buffer_store_b128(u32x4{x0[0], x1[0], x0[1], x1[1]}, rs, qb + m * 256 + (2 * (g & 1) + (g >> 1)) * 1024, WS_PQ, STM_Q);
      }
      {
        auto x0 = __builtin_amdgcn_permlane16_swap(pk[0][2][0], pk[1][2][0], false, false);
        auto x1 = __builtin_amdgcn_permlane16_swap(pk[0][2][1], pk[1][2][1], false, false);
        if (g < 2) __builtin_amdgcn_raw_buffer_store_b128(u32x4{x0[0], x1[0], x0[1], x1[1]}, rs, qb + (g & 1) * 256 + 4 * 1024, WS_PQ, STM_Q);
      }
      {
        const int t = (blockIdx.x * 4 + r) * 64 + lane;
        if (t < FL_BYTES / 16) __builtin_amdgcn_raw_buffer_store_b128(u32x4{0, 0, 0, 0}, rs, t * 16, WS_FL, STM_Q);
        else if (t < FL_BYTES / 16 + 320) {
          const int z = t - FL_BYTES / 16, q = z >> 2, k = z & 3;
          const unsigned o = k < 2 ? frag_off(q, 62 + k) : 33u * T640 + frag_off(q, k - 2);
          __builtin_amdgcn_raw_buffer_store_b128(u32x4{0, 0, 0, 0}, rs, o, WS_H1G, STM_Q);
        }
      }
      LBAR();
    } else {
      if (g == 0) {
#pragma unroll
        for (int u = 0; u < 5; u++) tcs[192 + r * 80 + u * 16 + i] = cs[u][0];
      }
      LBAR();
      TS(3);
      bf16x8 op[5];
      f32x4 m4[2], r4[2];
#pragma unroll
      for (int m = 0; m < 2; m++) { m4[m] = *(const f32x4*)(tmean + 32 * r + 16 * m + 4 * g); r4[m] = *(const f32x4*)(trstd + 32 * r + 16 * m + 4 * g); }
#pragma unroll
      for (int u = 0; u < 5; u++) {
        const float* p = tcs + 192 + u * 16 + i;
        const float c = (p[0] + p[80]) + (p[160] + p[240]);
        u32x4 o;
#pragma unroll
        for (int m = 0; m < 2; m++) {
          float v[4];
#pragma unroll
          for (int e = 0; e < 4; e++) {
            float a = r4[m][e] * (acc[m][u][e] - m4[m][e] * c);
            v[e] = u < 2 ? phi_f(a) : u == 2 ? (i < 8 ? phi_f(a) : a) : a;
          }
          o[2 * m] = cvtpk(v[0], v[1]); o[2 * m + 1] = cvtpk(v[2], v[3]);
        }
        op[u] = __builtin_bit_cast(bf16x8, o);
      }
      char* rw = red + r * 10240 + lane * 16;
      *(f32x4*)(rw + 0 * 1024) = mfma(op[0], op[3], z4);
      *(f32x4*)(rw + 1 * 1024) = mfma(op[0], op[4], z4);
      *(f32x4*)(rw + 2 * 1024) = mfma(op[1], op[3], z4);
      *(f32x4*)(rw + 3 * 1024) = mfma(op[1], op[4], z4);
      *(f32x4*)(rw + 4 * 1024) = mfma(op[1], op[2], z4);
      *(f32x4*)(rw + 5 * 1024) = mfma(op[2], op[4], z4);
      *(f32x4*)(rw + 6 * 1024) = mfma(op[2], op[2], z4);
      *(f32x4*)(rw + 7 * 1024) = mfma(op[0], ones, z4);
      *(f32x4*)(rw + 8 * 1024) = mfma(op[1], ones, z4);
      *(f32x4*)(rw + 9 * 1024) = mfma(op[2], ones, z4);
      LBAR();
      const int t = tid - 256;
      if (t < 210) {
        const int h = t >= 105, rem = t - 105 * h;
        const bool isz = rem >= 100;
        const int e = isz ? 0 : rem / 5, dq = isz ? rem - 100 : rem - e * 5;
        int dt, gg, et, ii;
        if (h == 0) { dt = dq < 4 ? 0 : 1; gg = dq < 4 ? dq : 0; et = e < 16 ? 0 : 1; ii = e & 15; }
        else { dt = dq < 3 ? 1 : 2; gg = dq < 3 ? dq + 1 : dq - 3; et = e < 12 ? 1 : 2; ii = e < 12 ? e + 4 : e - 4; }
        const int tile = isz ? 7 + dt : dt == 0 ? et : dt == 1 ? 2 + et : 4 + et;
        const char* p = red + tile * 1024 + (gg * 16 + (isz ? 0 : ii)) * 16;
        f32x4 s = *(const f32x4*)p;
#pragma unroll
        for (int k = 1; k < 4; k++) s += *(const f32x4*)(p + k * 10240);
        __builtin_amdgcn_raw_buffer_store_b128(__builtin_bit_cast(u32x4, s), rs, (unsigned)(((rt * 16 + hp) * 210 + t) * 16), WS_REC, STM_Q);
      }
    }
    TS(5);
  };
  if (w >= 4) role(IC<0>{}, IC<0>{});
  else if (K1_PT && blockIdx.x * 4 + r < PT_UNITS) role(IC<1>{}, IC<K1_PT>{});
  else role(IC<1>{}, IC<0>{});
  if (w == 0) TS_FLUSH(1, 0);
  if (w == 4) TS_FLUSH(1, 8);
}

typedef __attribute__((ext_vector_type(16))) float f32x16;
typedef GLB_AS unsigned gu32;
constexpr int LX_M = 131072, LX_A = 147456, L_ST = 159744, L_CS = 160768, L_T = 131072, CH_LDS = 162048;
#define CH_RD 28
#define CH_QZ 4
constexpr int RD = CH_RD, NFR = 154, QZ = CH_QZ;
constexpr int PBND[6] = {0, 20, 40, 60, 100, 154};
#define CH_NPRE 4
#define CH_SL 1
#define CH_DEN 2
#define CH_T2 88
constexpr int NPRE = CH_NPRE;
struct LSched { int step[NFR]; int pre[5]; int pub[5]; int pre2, pub2; };
constexpr LSched make_sched() {
  LSched s{};
  s.pre2 = 20 + NPRE; s.pub2 = RD < 40 ? RD : 40;
  int issued = s.pub2;
  constexpr int TG[5] = {40, 60, CH_T2, 128, 154};
  for (int p = 0; p < 5; p++) {
    for (int f = PBND[p]; f < PBND[p + 1]; f++) {
      const int den = p < 3 ? CH_DEN : 1;
      bool go = (f - PBND[p]) % den == 0 && issued < TG[p] && issued < f + 1 + RD;
      if (issued >= PBND[p + 1] && f >= PBND[p + 1] - QZ) go = false;
      if (issued < PBND[p + 1] && issued <= f + 4) go = true;
      if (go) issued++;
      s.step[f] = issued;
    }
    int lim = PBND[p + 1] + RD; if (lim > TG[p]) lim = TG[p];
    if (lim < issued) lim = issued;
    s.pre[p] = issued + NPRE < lim ? issued + NPRE : lim;
    issued = lim;
    s.pub[p] = issued;
  }
  return s;
}
constexpr LSched LS = make_sched();
constexpr int MON = 1;
constexpr int GON = 1;

__device__ __forceinline__ f32x16 mfma32(bf16x8 a, bf16x8 b, f32x16 c) {
  return __builtin_amdgcn_mfma_f32_32x32x16_bf16(a, b, c, 0, 0, 0);
}
template <int N> __device__ __forceinline__ void waitvmc() { __builtin_amdgcn_s_waitcnt((N & 15) | ((N >> 4) << 14) | 0x0F70); }
__device__ __forceinline__ void glds_pf(const char* sbase, unsigned voff, unsigned ldsoff) {
  asm volatile("s_mov_b32 m0, %2\n\tglobal_load_lds_dword %0, %1" ::"v"(voff), "s"(sbase), "s"(ldsoff) : "memory");
}
template <int N> __device__ __forceinline__ void waitbarv() { asm volatile("s_waitcnt vmcnt(%0)\n\ts_barrier" ::"n"(N) : "memory"); }
#define VM0() __builtin_amdgcn_s_waitcnt(0x0F70)
#define CTS_DECL
#define CTS(k)
#define CTS_FLUSH(base)

__global__ __launch_bounds__(512, 1) void k_chain(const bf16_t* __restrict__ x, char* __restrict__ ws, bf16_t* __restrict__ outp) {
  extern __shared__ __attribute__((aligned(16))) char lds[];
  const int tid = threadIdx.x, lane = tid & 63;
  const int w = __builtin_amdgcn_readfirstlane(tid >> 6);
  const int xcd = blockIdx.x & 7, idx = blockIdx.x >> 3;
  const int rt = xcd * 4 + (idx >> 3), cb = idx & 7;
  const __amdgpu_buffer_rsrc_t rs = __builtin_amdgcn_make_buffer_rsrc(ws, 0, 0x10000000, 0x00020000);
  auto bload = [&](unsigned v, unsigned so) { return __builtin_bit_cast(bf16x8, __builtin_amdgcn_raw_buffer_load_b128(rs, v, so, 0)); };
  auto bst16 = [&](unsigned v, unsigned so, u32x4 d) { __builtin_amdgcn_raw_buffer_store_b128(d, rs, v, so, 16); };
  auto bst8 = [&](unsigned v, unsigned so, u32x2 d) { __builtin_amdgcn_raw_buffer_store_b64(d, rs, v, so, 16); };
  CTS_DECL;
  CTS(0);

  f32x4 rv[20];
  bf16x8 bq[3];
  int rn = 16;
  {
    const int hpl = tid >= 210;
    unsigned rb = (unsigned)WS_REC + (unsigned)(((2 * cb + hpl) * 210 + tid - 210 * hpl) * 16), rst = 53760;
    if (tid >= 420) {
      rn = tid < 440 || (tid >= 448 && tid < 502) ? 20 : 0;
      rst = rn == 0 ? 0 : tid < 448 ? 2560 : 6912;
      rb = rn == 0 ? (unsigned)WS_REC : tid < 448 ? (unsigned)WS_CSP1 + (cb * 80 + 4 * (tid - 420)) * 4 : (unsigned)WS_CSP3 + (cb * 216 + 4 * (tid - 448)) * 4;
    }
#pragma unroll
    for (int p = 0; p < 16; p++) rv[p] = __builtin_bit_cast(f32x4, __builtin_amdgcn_raw_buffer_load_b128(rs, rb + p * rst, 0, 0));
    if (w >= 6) {
#pragma unroll
      for (int p = 16; p < 20; p++) rv[p] = __builtin_bit_cast(f32x4, __builtin_amdgcn_raw_buffer_load_b128(rs, rb + (rn == 20 ? p * rst : 0), 0, 0));
    }
    const int g = lane >> 4;
#pragma unroll
    for (int s = 0; s < 3; s++) {
      const int c = 4 * s + g < 9 ? 4 * s + g : 9;
      bq[s] = bload((unsigned)((rt * 8 + cb) * 10240 + c * 1024 + (16 * (w & 3) + (lane & 15)) * 16), WS_PQ);
    }
    CBAR();
#pragma unroll
    for (int k = 0; k < 3; k++)
      if (tid + 512 * k < 1092) *(LDS_AS u32x4*)(lds + L_T + (tid + 512 * k) * 16) = u32x4{0, 0, 0, 0};
  }
  auto p2 = [&]() {
    const int i = lane & 15, g = lane >> 4;
    LBAR();
    f32x4 s = ((rv[0] + rv[1]) + (rv[2] + rv[3])) + ((rv[4] + rv[5]) + (rv[6] + rv[7])) +
              (((rv[8] + rv[9]) + (rv[10] + rv[11])) + ((rv[12] + rv[13]) + (rv[14] + rv[15])));
    if (w >= 6) { if (rn == 20) s += (rv[16] + rv[17]) + (rv[18] + rv[19]); }
    CTS(24);
    if (tid < 420) {
      const int hpl = tid >= 210, t = tid - 210 * hpl, h = t >= 105, rem = t - 105 * h, hl = 2 * hpl + h;
      const int e = rem / 5, dq = rem - e * 5;
      const int n = e == 20 ? 80 + hl : hl * 20 + e;
      *(LDS_AS u32x2*)(lds + L_T + n * 208 + (hl * 20 + 4 * dq) * 2) = u32x2{cvtpk(s[0], s[1]), cvtpk(s[2], s[3])};
    } else if (rn == 20) {
      *(LDS_AS f32x4*)(lds + L_CS + (tid < 448 ? (tid - 420) * 16 : 320 + (tid - 448) * 16)) = s;
    }
    LBAR();
    const char* tp = lds + L_T + i * 208 + g * 16;
    const char* zp = lds + L_T + (80 + (i & 3)) * 208 + g * 16;
    const f32x4 z4 = {0, 0, 0, 0};
    f32x4 dn = mfma(ldsfrag(zp), bq[0], z4);
    dn = mfma(ldsfrag(zp + 64), bq[1], dn);
    dn = mfma(ldsfrag(zp + 128), bq[2], dn);
    const unsigned so = (unsigned)(rt * T640) + frag_off(cb * 10 + (g >> 1), 16 * (w & 3) + i) + (g & 1) * 8;
    auto fin = [&](auto UI, const f32x4& nu) {
      constexpr int u = decltype(UI)::value;
      const float d = u == 0 ? dn[0] : u == 1 ? (g == 0 ? dn[0] : dn[1]) : u == 2 ? (g < 2 ? dn[1] : dn[2]) : u == 3 ? (g < 3 ? dn[2] : dn[3]) : dn[3];
      const float inv = __builtin_amdgcn_rcpf(d + 1e-6f);
      const unsigned o = (unsigned)(rt * T640) + frag_off(cb * 10 + 2 * u + (g >> 1), 16 * (w & 3) + i) + (g & 1) * 8;
      bst8(o, WS_O, u32x2{cvtpk(nu[0] * inv, nu[1] * inv), cvtpk(nu[2] * inv, nu[3] * inv)});
    };
    (void)so;
    if (w < 4) {
      f32x4 n0 = mfma(ldsfrag(tp), bq[0], z4);
      f32x4 n1 = mfma(ldsfrag(tp + 16 * 208), bq[0], z4);
      f32x4 n2 = mfma(ldsfrag(tp + 32 * 208), bq[0], z4);
      n1 = mfma(ldsfrag(tp + 16 * 208 + 64), bq[1], n1);
      n2 = mfma(ldsfrag(tp + 32 * 208 + 64), bq[1], n2);
      fin(IC<0>{}, n0); fin(IC<1>{}, n1); fin(IC<2>{}, n2);
    } else {
      f32x4 n3 = mfma(ldsfrag(tp + 48 * 208 + 64), bq[1], z4);
      f32x4 n4 = mfma(ldsfrag(tp + 64 * 208 + 64), bq[1], z4);
      n3 = mfma(ldsfrag(tp + 48 * 208 + 128), bq[2], n3);
      n4 = mfma(ldsfrag(tp + 64 * 208 + 128), bq[2], n4);
      fin(IC<3>{}, n3); fin(IC<4>{}, n4);
    }
    CTS(25);
  };

  if (w < 4) {
    const int t = w & 1, kh = w >> 1, hh = lane >> 5, l31 = lane & 31;
    const unsigned vA = cb * 102400 + kh * 5120 + t * 2048 + lane * 16;
    const unsigned vB = cb * 286720 + w * 2048 + lane * 16;
    const unsigned vC = cb * 276480 + kh * 5120 + t * 2048 + lane * 16;
    bf16x8 ring[RD];
    auto wl = [&](auto FI) {
      constexpr int F = decltype(FI)::value;
      constexpr int ph = F < 20 ? 0 : F < 40 ? 1 : F < 60 ? 2 : F < 100 ? 3 : 4;
      constexpr int f = F - PBND[ph];
      constexpr unsigned so = ph == 0 ? WS_WOT + (f >> 1) * 10240 : ph == 1 ? WS_W1T + (f >> 1) * 10240 : ph == 2 ? WS_W2T + (f >> 1) * 10240
                            : ph == 3 ? WS_W3T + (f >> 1) * 14336 : WS_W4T + (f >> 1) * 10240;
      ring[F % RD] = bload((ph < 3 ? vA : ph == 3 ? vB : vC) + (f & 1) * 1024, so);
    };
    sfor<0, 20>(wl);
    const int row = 32 * kh + l31;
    const int colq = cb * 10 + 4 * t + hh;
    u32x4 hr[2], x1r[2];
    {
      const bf16_t* xp = x + (size_t)(rt * 64 + row) * D_ + colq * 8;
      hr[0] = *(const u32x4*)xp; hr[1] = *(const u32x4*)(xp + 16);
    }
    const unsigned so640[2] = {(unsigned)(rt * T640) + frag_off(colq, row), (unsigned)(rt * T640) + frag_off(colq + 2, row)};
    const unsigned lp = ((lane >> 4) & 1) * 1024 + hh * 256 + (lane & 15) * 16;
    const char* aA[2] = {lds + kh * 4096 + kh * 2048 + lp, lds + kh * 4096 + (kh ^ 1) * 2048 + lp};
    const char* aL = lds + lp;
    f32x16 acc[2];
    bf16x8 af[3][2];
    float mean1 = 0.f, r1 = 0.f;

    auto gemm = [&](auto PH) {
      constexpr int ph = decltype(PH)::value;
      constexpr int F0 = PBND[ph], NS = PBND[ph + 1] - F0;
      auto rdA = [&](auto SI) {
        constexpr int s = decltype(SI)::value;
        if constexpr (ph == 3) {
          constexpr int o = (s >> 1) * 4096 + (s & 1) * 512;
          af[s % 3][0] = ldsfrag(aL + o); af[s % 3][1] = ldsfrag(aL + o + 2048);
        } else {
          constexpr int o = (ph == 4 ? ((s >> 2) & 7) * 16384 + ((s >> 1) & 1) * 8192 : (s >> 1) * 8192) + (s & 1) * 512;
          af[s % 3][0] = ldsfrag(aA[0] + o); af[s % 3][1] = ldsfrag(aA[1] + o);
        }
      };
#pragma unroll
      for (int e = 0; e < 16; e++) { acc[0][e] = 0.f; acc[1][e] = 0.f; }
      SBAR();
      CTS(1 + 4 * ph);
      rdA(IC<0>{}); rdA(IC<1>{});
      __builtin_amdgcn_sched_barrier(0);
      sfor<0, NS>([&](auto SI) {
        constexpr int s = decltype(SI)::value;
        if constexpr (s + 2 < NS) {
          if constexpr (((s + 2) & (ph == 3 ? 7 : 3)) == 0) SBAR();
          rdA(IC<s + 2>{});
        }
        acc[0] = mfma32(ring[(F0 + s) % RD], af[s % 3][0], acc[0]);
        acc[1] = mfma32(ring[(F0 + s) % RD], af[s % 3][1], acc[1]);
        constexpr int l0 = (F0 + s) == 0 ? LS.pub2 : (s == 0 ? LS.pub[ph > 0 ? ph - 1 : 0] : LS.step[F0 + s - (F0 + s > 0 ? 1 : 0)]);
        sfor<l0, LS.step[F0 + s]>(wl);
        __builtin_amdgcn_sched_barrier(0);
      });
      CTS(2 + 4 * ph);
    };
    auto exch = [&]() {
      char* xw = lds + LX_M + w * 4096 + lane * 16;
      const char* xr = lds + LX_M + (w ^ 2) * 4096 + lane * 16;
#pragma unroll
      for (int j = 0; j < 4; j++) *(LDS_AS f32x4*)(xw + j * 1024) = f32x4{acc[1][4 * j], acc[1][4 * j + 1], acc[1][4 * j + 2], acc[1][4 * j + 3]};
      LBAR();
#pragma unroll
      for (int j = 0; j < 4; j++) {
        f32x4 v = *(const LDS_AS f32x4*)(xr + j * 1024);
#pragma unroll
        for (int e = 0; e < 4; e++) acc[0][4 * j + e] += v[e];
      }
    };
    auto wlp = [&](auto FI) { wl(FI); __builtin_amdgcn_s_sleep(CH_SL); __builtin_amdgcn_sched_barrier(0); };
    auto publish = [&](auto PH) {
      constexpr int ph = decltype(PH)::value;
      CTS(3 + 4 * ph);
      __builtin_amdgcn_sched_barrier(0);
      sfor<LS.step[PBND[ph + 1] - 1], LS.pre[ph]>(wlp);
      waitvmc<MON * (LS.pre[ph] - LS.step[PBND[ph + 1] - 1])>();
      SBAR();
      CTS(4 + 4 * ph);
      sfor<LS.pre[ph], LS.pub[ph]>(wlp);
      __builtin_amdgcn_sched_barrier(0);
    };
    auto res = [&](const u32x4& r, int e) { return (e & 1) ? bfhi(r[e >> 1]) : bflo(r[e >> 1]); };

    p2();
    __builtin_amdgcn_sched_barrier(0);
    sfor<20, LS.pre2>(wlp);
    waitvmc<MON * (LS.pre2 - 20)>();
    SBAR();
    CTS(26);
    sfor<LS.pre2, LS.pub2>(wlp);
    __builtin_amdgcn_sched_barrier(0);
    gemm(IC<0>{});
    exch();
#pragma unroll
    for (int c = 0; c < 2; c++) {
#pragma unroll
      for (int d = 0; d < 4; d++)
        hr[c][d] = cvtpk(res(hr[c], 2 * d) + acc[0][8 * c + 2 * d], res(hr[c], 2 * d + 1) + acc[0][8 * c + 2 * d + 1]);
      bst16(so640[c], WS_H, hr[c]);
    }
    publish(IC<0>{});
    gemm(IC<1>{});
    exch();
    {
      f32x4 st = *(const LDS_AS f32x4*)(lds + L_ST + row * 16);
      mean1 = st[0]; r1 = st[1];
      const float rs1 = st[2];
#pragma unroll
      for (int c = 0; c < 2; c++) {
        const char* cp = lds + L_CS + (32 * t + 16 * c + 8 * hh) * 4;
        f32x4 c0 = *(const LDS_AS f32x4*)cp, c1 = *(const LDS_AS f32x4*)(cp + 16);
        float v[8];
#pragma unroll
        for (int e = 0; e < 8; e++) v[e] = gelu_f(rs1 * (acc[0][8 * c + e] - mean1 * (e < 4 ? c0[e & 3] : c1[e & 3])));
        bst16(so640[c], WS_H1, u32x4{cvtpk(v[0], v[1]), cvtpk(v[2], v[3]), cvtpk(v[4], v[5]), cvtpk(v[6], v[7])});
      }
    }
    publish(IC<1>{});
    gemm(IC<2>{});
    exch();
#pragma unroll
    for (int c = 0; c < 2; c++) {
#pragma unroll
      for (int d = 0; d < 4; d++)
        x1r[c][d] = cvtpk((res(hr[c], 2 * d) - mean1) * r1 + acc[0][8 * c + 2 * d], (res(hr[c], 2 * d + 1) - mean1) * r1 + acc[0][8 * c + 2 * d + 1]);
      bst16(so640[c], WS_X1, x1r[c]);
    }
    publish(IC<2>{});
    gemm(IC<3>{});
    LBAR();
    {
      const int q0 = cb * 27 + 4 * w + hh;
#pragma unroll
      for (int c = 0; c < 2; c++) {
        const char* cp = lds + L_CS + 320 + (32 * w + 16 * c + 8 * hh) * 4;
        f32x4 c0 = *(const LDS_AS f32x4*)cp, c1 = *(const LDS_AS f32x4*)(cp + 16);
#pragma unroll
        for (int p = 0; p < 2; p++) {
          f32x4 st = *(const LDS_AS f32x4*)(lds + L_ST + (32 * p + l31) * 16);
          float v[8];
#pragma unroll
          for (int e = 0; e < 8; e++) v[e] = gelu_f(st[1] * (acc[p][8 * c + e] - st[0] * (e < 4 ? c0[e & 3] : c1[e & 3])));
          bst16((unsigned)(rt * T1728) + frag_off(q0 + 2 * c, 32 * p + l31), WS_H2,
                u32x4{cvtpk(v[0], v[1]), cvtpk(v[2], v[3]), cvtpk(v[4], v[5]), cvtpk(v[6], v[7])});
        }
      }
    }
    publish(IC<3>{});
    gemm(IC<4>{});
    exch();
    {
      int ln = lane; asm volatile("" : "+v"(ln));
      bf16_t* op = outp + (size_t)(rt * 64 + 32 * kh + (ln & 31)) * D_ + (cb * 10 + 4 * t + (ln >> 5)) * 8;
#pragma unroll
      for (int c = 0; c < 2; c++) {
        u32x4 o;
#pragma unroll
        for (int d = 0; d < 4; d++)
          o[d] = cvtpk(res(hr[c], 2 * d) + res(x1r[c], 2 * d) + acc[0][8 * c + 2 * d],
                       res(hr[c], 2 * d + 1) + res(x1r[c], 2 * d + 1) + acc[0][8 * c + 2 * d + 1]);
        *(u32x4*)(op + 16 * c) = o;
      }
    }
    CTS(19); CTS(20); CTS(21); CTS(22); CTS(23); CTS(27);
    if (w == 0) CTS_FLUSH(0);
    return;
  }

  const int a = w - 4, i = lane & 15, g = lane >> 4;
  const int row = 16 * a + i;
  bf16x8 G[59];
  const unsigned vG = cb * 102400 + a * 5120 + 4096 + lane * 16;
  const unsigned vG7 = cb * 276480 + a * 5120 + 4096 + lane * 16;
  const int b = a & 1;
  const unsigned vE = cb * 286720 + b * 14336 + (8 + a) * 1024 + lane * 16;
  const unsigned vO = cb * 286720 + (1 - b) * 14336 + (8 + a) * 1024 + lane * 16;
  const unsigned vS = cb * 286720 + b * 14336 + (12 + (a >> 1)) * 1024 + lane * 16;
  auto gl = [&](auto GI) {
    constexpr int n = decltype(GI)::value;
    if constexpr (GON == 0) { bf16x8 tmp_; asm volatile("" : "=v"(tmp_)); G[n] = tmp_; }
    else if constexpr (n < 5) G[n] = bload(vG, WS_WOT + n * 20480);
    else if constexpr (n < 10) G[n] = bload(vG, WS_W1T + (n - 5) * 20480);
    else if constexpr (n < 15) G[n] = bload(vG, WS_W2T + (n - 10) * 20480);
    else if constexpr (n < 45) {
      constexpr int c = (n - 15) / 6, k = (n - 15) % 6;
      if constexpr (k < 4) G[n] = bload((k & 1) ? vO : vE, WS_W3T + (4 * c + (k & ~1)) * 14336);
      else G[n] = bload(vS, WS_W3T + (4 * c + 2 * (k - 4)) * 14336);
    } else G[n] = bload(vG7, WS_W4T + (n - 45) * 20480);
  };
  const unsigned vT = rt * T640 + a * 1024 + lane * 16;
#define CH_GS {{9, 11, 12, 13, 15, 18}, {20, 22, 24, 26, 28, 31}, {33, 35, 37, 39, 41, 43}, {45, 48, 51, 55, 59, 59}}
  constexpr int GS[4][6] = CH_GS;
  constexpr int G2 = 7;
  u32x2 hr, x1r;
  hr = *(const u32x2*)(x + (size_t)(rt * 64 + row) * D_ + cb * 80 + 64 + 4 * g);
  sfor<0, 5>(gl);
  __builtin_amdgcn_sched_barrier(0);
  const unsigned so640 = rt * T640 + frag_off(cb * 10 + 8 + (g >> 1), row) + (g & 1) * 8;
  float mean1 = 0.f, r1 = 0.f;
  const char* aR[4];
#pragma unroll
  for (int p = 0; p < 4; p++) aR[p] = lds + a * 4096 + (a ^ p) * 1024 + lane * 16;
  const char* aSt[3];
#pragma unroll
  for (int d = 1; d < 4; d++) aSt[d - 1] = lds + (a ^ d) * 4096 + a * 1024 + lane * 16;
  gu32* flags = (gu32*)(ws + WS_FL);
  auto poll = [&](int ph) {
    int r2 = rt, wd = lane & 7;
    bool need = lane < 8;
    if (ph == 2) {
      if (lane >= 8 && lane < 16 && rt > 0 && (wd & 2) == 0) { r2 = rt - 1; need = true; }
      if (lane >= 16 && lane < 24 && rt < 31 && (wd & 2) != 0) { r2 = rt + 1; need = true; }
    }
    gu32* p = flags + (ph * 32 + r2) * 64 + wd;
#pragma unroll 1
    for (unsigned spins = 0; spins < (1u << 18); spins++) {
      unsigned v = need ? __hip_atomic_load(p, __ATOMIC_RELAXED, __HIP_MEMORY_SCOPE_AGENT) : 1u;
      if (__all(v == 1u)) break;
      __builtin_amdgcn_s_sleep(1);
    }
  };
  auto publish = [&](auto PH) {
    constexpr int ph = decltype(PH)::value;
    CTS(3 + 4 * ph);
    __builtin_amdgcn_sched_barrier(0);
    sfor<GS[ph][4], GS[ph][5]>(gl);
    waitvmc<GON * (GS[ph][5] - GS[ph][4])>();
    SBAR();
    CTS(4 + 4 * ph);
    if (tid == 256) __hip_atomic_store(flags + ((ph + 1) * 32 + rt) * 64 + cb, 1u, __ATOMIC_RELAXED, __HIP_MEMORY_SCOPE_AGENT);
  };
  auto stat_acc = [&](const bf16x8& f, float& s, float& q) {
    u32x4 u = __builtin_bit_cast(u32x4, f);
#pragma unroll
    for (int d = 0; d < 4; d++) { s = dot2(u[d], 0x3F803F80u, s); q = dot2(u[d], u[d], q); }
  };
  auto stat_fin = [&](float s, float q) {
    s = xsum4(s); q = xsum4(q);
    float mean = s * (1.0f / D_);
    float var = fmaxf(q * (1.0f / D_) - mean * mean, 0.f);
    float ra = __builtin_amdgcn_rsqf(var + 1e-5f);
    float rb = ra * __builtin_amdgcn_rsqf(var * ra * ra + 1e-5f);
    if (g == 0) *(LDS_AS f32x4*)(lds + L_ST + row * 16) = f32x4{mean, ra, rb, 0.f};
    mean1 = mean; r1 = ra;
    return rb;
  };
  f32x4 ac[4];
  auto exch = [&]() {
#pragma unroll
    for (int p = 1; p < 4; p++) *(LDS_AS f32x4*)(lds + LX_A + (a ^ p) * 3072 + (p - 1) * 1024 + lane * 16) = ac[p];
    LBAR();
#pragma unroll
    for (int p = 1; p < 4; p++) ac[0] += *(const LDS_AS f32x4*)(lds + LX_A + a * 3072 + (p - 1) * 1024 + lane * 16);
  };
  auto tile4 = [&](auto PH, float& ss, float& sq) {
    constexpr int ph = decltype(PH)::value;
    constexpr int G0 = ph == 0 ? G2 : GS[ph - 1][5], PC = 4;
#pragma unroll
    for (int p = 0; p < 4; p++) ac[p] = f32x4{0, 0, 0, 0};
    sfor<0, 5>([&](auto CI) {
      constexpr int c = decltype(CI)::value;
      constexpr int GP = c == 0 ? G0 : GS[ph][c - 1];
      waitbarv<PC * (4 - c) + GON * (GP - G0)>();
      bf16x8 f[4], wf;
#pragma unroll
      for (int p = 0; p < 4; p++) f[p] = ldsfrag(aR[p] + c * 16384);
      wf = G[ph * 5 + c];
      bf16x8 e[3];
      if constexpr (ph == 1) {
#pragma unroll
        for (int d = 0; d < 3; d++) e[d] = ldsfrag(aSt[d] + c * 16384);
      }
      sfor<GP, GS[ph][c]>(gl);
#pragma unroll
      for (int p = 0; p < 4; p++) ac[p] = mfma(wf, f[p], ac[p]);
      if constexpr (ph == 1) {
        stat_acc(f[0], ss, sq);
#pragma unroll
        for (int d = 0; d < 3; d++) stat_acc(e[d], ss, sq);
      }
      __builtin_amdgcn_sched_barrier(0);
    });
  };
  float ss = 0.f, sq = 0.f;
  p2();
  __builtin_amdgcn_sched_barrier(0);
  sfor<5, G2>(gl);
  waitvmc<GON * (G2 - 5)>();
  SBAR();
  CTS(26);
  if (tid == 256) __hip_atomic_store(flags + rt * 64 + cb, 1u, __ATOMIC_RELAXED, __HIP_MEMORY_SCOPE_AGENT);
  poll(0);
  CTS(1);
  sfor<0, 20>([&](auto KI) { constexpr int ks = decltype(KI)::value; glds_sc1(ws + WS_O + ks * 4096, vT, ks * 4096 + a * 1024); });
  tile4(IC<0>{}, ss, sq);
  CTS(2);
  exch();
#pragma unroll
  for (int d = 0; d < 2; d++) hr[d] = cvtpk(bflo(hr[d]) + ac[0][2 * d], bfhi(hr[d]) + ac[0][2 * d + 1]);
  bst8(so640, WS_H, hr);
  publish(IC<0>{});
  poll(1);
  CTS(5);
  sfor<0, 20>([&](auto KI) { constexpr int ks = decltype(KI)::value; glds_sc1(ws + WS_H + ks * 4096, vT, ks * 4096 + a * 1024); });
  tile4(IC<1>{}, ss, sq);
  CTS(6);
  {
    float rs1 = stat_fin(ss, sq);
    exch();
    f32x4 c4 = *(const LDS_AS f32x4*)(lds + L_CS + (64 + 4 * g) * 4);
    float v[4];
#pragma unroll
    for (int e = 0; e < 4; e++) v[e] = gelu_f(rs1 * (ac[0][e] - mean1 * c4[e]));
    bst8(so640, WS_H1, u32x2{cvtpk(v[0], v[1]), cvtpk(v[2], v[3])});
  }
  publish(IC<1>{});
  {
    unsigned vSh[4];
#pragma unroll
    for (int k = 0; k < 4; k++) {
      const int sh = k == 0 ? 1 : k == 1 ? -1 : k == 2 ? 2 : -2;
      int r = row - sh + 64;
      vSh[k] = (unsigned)((rt + (r >> 6)) * T640 + ((r >> 4) & 3) * 1024 + (r & 15) * 16 + g * 256);
    }
    poll(2);
    CTS(9);
    sfor<0, 20>([&](auto KI) { constexpr int ks = decltype(KI)::value; glds_sc1(ws + WS_H1G + ks * 4096, vSh[ks / 5], ks * 4096 + a * 1024); });
  }
  tile4(IC<2>{}, ss, sq);
  CTS(10);
  exch();
#pragma unroll
  for (int d = 0; d < 2; d++)
    x1r[d] = cvtpk((bflo(hr[d]) - mean1) * r1 + ac[0][2 * d], (bfhi(hr[d]) - mean1) * r1 + ac[0][2 * d + 1]);
  bst8(so640, WS_X1, x1r);
  publish(IC<2>{});
  poll(3);
  CTS(13);
  sfor<0, 20>([&](auto KI) { constexpr int ks = decltype(KI)::value; glds_sc1(ws + WS_X1 + ks * 4096, vT, ks * 4096 + a * 1024); });
  {
    f32x4 ao[4], as[4];
#pragma unroll
    for (int u = 0; u < 4; u++) { ao[u] = f32x4{0, 0, 0, 0}; as[u] = f32x4{0, 0, 0, 0}; }
    const char *lE[4], *lO[4];
#pragma unroll
    for (int u = 0; u < 4; u++) {
      lE[u] = lds + b * 4096 + (u ^ a) * 1024 + lane * 16;
      lO[u] = lds + (1 - b) * 4096 + (u ^ a) * 1024 + lane * 16;
    }
    ss = 0.f; sq = 0.f;
    sfor<0, 5>([&](auto CI) {
      constexpr int c = decltype(CI)::value;
      constexpr int GP = c == 0 ? GS[2][5] : GS[3][c - 1];
      waitbarv<4 * (4 - c) + GON * (GP - GS[2][5])>();
      sfor<0, 4>([&](auto PI) {
        constexpr int p = decltype(PI)::value;
        bf16x8 f[4];
#pragma unroll
        for (int u = 0; u < 4; u++) f[u] = ldsfrag(((p & 1) ? lO[u] : lE[u]) + (4 * c + (p & ~1)) * 4096);
#pragma unroll
        for (int u = 0; u < 4; u++) ao[u] = mfma(G[15 + 6 * c + p], f[u], ao[u]);
        if constexpr ((p & 1) == 0) {
#pragma unroll
          for (int u = 0; u < 4; u++) as[u] = mfma(G[15 + 6 * c + 4 + (p >> 1)], f[u], as[u]);
        }
        stat_acc(f[0], ss, sq);
      });
      __builtin_amdgcn_sched_barrier(0);
      sfor<GP, GS[3][c]>(gl);
      __builtin_amdgcn_sched_barrier(0);
    });
    CTS(14);
    stat_fin(ss, sq);
    *(LDS_AS f32x4*)(lds + LX_A + (a ^ 1) * 3072 + lane * 16) = as[1];
    *(LDS_AS f32x4*)(lds + LX_A + (a ^ 1) * 3072 + 1024 + lane * 16) = as[3];
    LBAR();
    as[0] += *(const LDS_AS f32x4*)(lds + LX_A + a * 3072 + lane * 16);
    as[2] += *(const LDS_AS f32x4*)(lds + LX_A + a * 3072 + 1024 + lane * 16);
    int ln = lane; asm volatile("" : "+v"(ln));
    const int i = ln & 15, g = ln >> 4;
    const f32x4 co = *(const LDS_AS f32x4*)(lds + L_CS + 320 + (128 + 16 * a + 4 * g) * 4);
    const int st = 12 + (a >> 1);
    const bool shv = !(st == 13 && g >= 2);
    const f32x4 cs = *(const LDS_AS f32x4*)(lds + L_CS + 320 + (shv ? (16 * st + 4 * g) * 4 : 0));
    const int qo = cb * 27 + 16 + 2 * a + (g >> 1), qs = cb * 27 + 16 + 2 * (st - 8) + (g >> 1);
#pragma unroll
    for (int u = 0; u < 4; u++) {
      const int r = (u ^ a) * 16 + i;
      f32x4 s4 = *(const LDS_AS f32x4*)(lds + L_ST + r * 16);
      float v[4];
#pragma unroll
      for (int e = 0; e < 4; e++) v[e] = gelu_f(s4[1] * (ao[u][e] - s4[0] * co[e]));
      bst8((unsigned)(rt * T1728) + frag_off(qo, r) + (g & 1) * 8, WS_H2, u32x2{cvtpk(v[0], v[1]), cvtpk(v[2], v[3])});
      if ((u & 1) == 0) {
#pragma unroll
        for (int e = 0; e < 4; e++) v[e] = gelu_f(s4[1] * (as[u][e] - s4[0] * cs[e]));
        if (shv) bst8((unsigned)(rt * T1728) + frag_off(qs, r) + (g & 1) * 8, WS_H2, u32x2{cvtpk(v[0], v[1]), cvtpk(v[2], v[3])});
      }
    }
  }
  publish(IC<3>{});
  poll(4);
  CTS(17);
  {
    const unsigned vT7 = rt * T1728 + a * 1024 + lane * 16;
    auto issue = [&](auto CI) {
      constexpr int c = decltype(CI)::value;
      if constexpr (c < 14) {
        sfor<0, (c == 13 ? 2 : 4)>([&](auto KI) {
          constexpr int k = decltype(KI)::value;
          glds_sc1(ws + WS_H2 + (4 * c + k) * 4096, vT7, (c & 7) * 16384 + k * 4096 + a * 1024);
        });
      }
    };
    sfor<0, 6>(issue);
#pragma unroll
    for (int p = 0; p < 4; p++) ac[p] = f32x4{0, 0, 0, 0};
    sfor<0, 14>([&](auto CI) {
      constexpr int c = decltype(CI)::value;
      constexpr int last = c + 5 > 13 ? 13 : c + 5;
      constexpr int young = (last - c) * 4 - (last == 13 && c < 13 ? 2 : 0);
      waitbarv<young>();
      issue(IC<c + 6>{});
      if (c < 13 || a < 2) {
        bf16x8 f[4];
#pragma unroll
        for (int p = 0; p < 4; p++) f[p] = ldsfrag(aR[p] + (c & 7) * 16384);
#pragma unroll
        for (int p = 0; p < 4; p++) ac[p] = mfma(G[45 + c], f[p], ac[p]);
      }
      __builtin_amdgcn_sched_barrier(0);
    });
  }
  CTS(18);
  exch();
  {
    u32x2 o;
#pragma unroll
    for (int d = 0; d < 2; d++) o[d] = cvtpk(bflo(hr[d]) + bflo(x1r[d]) + ac[0][2 * d], bfhi(hr[d]) + bfhi(x1r[d]) + ac[0][2 * d + 1]);
    int ln = lane; asm volatile("" : "+v"(ln));
    *(u32x2*)(outp + (size_t)(rt * 64 + 16 * a + (ln & 15)) * D_ + cb * 80 + 64 + 4 * (ln >> 4)) = o;
  }
  CTS(19); CTS(20); CTS(21); CTS(22); CTS(23); CTS(27);
  if (w == 4) CTS_FLUSH(32);
}

extern "C" void kernel_launch(void* const* d_in, const int* in_sizes, int n_in, void* d_out, int out_size, void* d_ws,
                              size_t ws_size, hipStream_t stream) {
  const bf16_t* x = (const bf16_t*)d_in[0];
  const bf16_t* wqkv = (const bf16_t*)d_in[1];
  const bf16_t* wo = (const bf16_t*)d_in[2];
  const bf16_t* w1 = (const bf16_t*)d_in[4];
  const bf16_t* w2 = (const bf16_t*)d_in[5];
  const bf16_t* w3 = (const bf16_t*)d_in[6];
  const bf16_t* w4 = (const bf16_t*)d_in[7];
  char* ws = (char*)d_ws;
  bf16_t* out = (bf16_t*)d_out;
  (void)hipFuncSetAttribute((const void*)k_qkv, hipFuncAttributeMaxDynamicSharedMemorySize, K1_LDS);
  (void)hipFuncSetAttribute((const void*)k_chain, hipFuncAttributeMaxDynamicSharedMemorySize, CH_LDS);
  k_qkv<<<256, 512, K1_LDS, stream>>>(x, wqkv, wo, w1, w2, w3, w4, ws);
  k_chain<<<256, 512, CH_LDS, stream>>>(x, ws, out);
}
```

```cpp
#include <hip/hip_runtime.h>
#include <stdint.h>

typedef unsigned short bf16_t;
typedef __attribute__((ext_vector_type(8))) short bf16x8;
typedef __attribute__((ext_vector_type(4))) short s16x4;
typedef __attribute__((ext_vector_type(4))) float f32x4;
typedef __attribute__((ext_vector_type(2))) float f32x2;
typedef __attribute__((ext_vector_type(4))) unsigned u32x4;
typedef __attribute__((ext_vector_type(2))) unsigned u32x2;
#define LDS_AS __attribute__((address_space(3)))
#define GLB_AS __attribute__((address_space(1)))

constexpr int S_ = 2048, D_ = 640, F_ = 1728;

constexpr size_t MB_ = 1u << 20;
constexpr size_t WS_FL = 0, WS_REC = 65536, WS_CSP1 = 925696, WS_CSP3 = 976896,
                 WS_WOT = 1179648, WS_W1T = 1998848, WS_W2T = 2818048, WS_W3T = 3637248, WS_W4T = 5931008,
                 WS_PQ = 8142848, WS_O = 10764288, WS_H = 13385728,
                 WS_H1G = 16007168,
                 WS_H1 = WS_H1G + 81920, WS_X1 = 18792448, WS_H2 = 21413888;
constexpr int FL_BYTES = 49152;
constexpr int T640 = 81920, T1728 = 221184;
__device__ __forceinline__ unsigned frag_off(int q, int r) { return (unsigned)((q >> 2) * 4096 + (r >> 4) * 1024 + (q & 3) * 256 + (r & 15) * 16); }

#define STM_A 0
#define STM_X 0

template <int MODE> __device__ __forceinline__ void st16(void* p, u32x4 v) {
  if (MODE == 0) *(u32x4*)p = v;
  else if (MODE == 1) __builtin_nontemporal_store(v, (u32x4*)p);
  else if (MODE == 2) asm volatile("global_store_dwordx4 %0, %1, off sc0 sc1" ::"v"(p), "v"(v) : "memory");
  else asm volatile("global_store_dwordx4 %0, %1, off sc0 sc1 nt" ::"v"(p), "v"(v) : "memory");
}
template <int MODE> __device__ __forceinline__ void st8(void* p, u32x2 v) {
  if (MODE == 0) *(u32x2*)p = v;
  else if (MODE == 1) __builtin_nontemporal_store(v, (u32x2*)p);
  else if (MODE == 2) asm volatile("global_store_dwordx2 %0, %1, off sc0 sc1" ::"v"(p), "v"(v) : "memory");
  else asm volatile("global_store_dwordx2 %0, %1, off sc0 sc1 nt" ::"v"(p), "v"(v) : "memory");
}

__device__ __forceinline__ unsigned cvtpk(float lo, float hi) {
  unsigned r; asm("v_cvt_pk_bf16_f32 %0, %1, %2" : "=v"(r) : "v"(lo), "v"(hi)); return r;
}
__device__ __forceinline__ float bflo(unsigned v) { return __uint_as_float(v << 16); }
__device__ __forceinline__ float bfhi(unsigned v) { return __uint_as_float(v & 0xffff0000u); }
__device__ __forceinline__ void glds16(const void* g, char* l) {
  __builtin_amdgcn_global_load_lds((const GLB_AS void*)g, (LDS_AS void*)l, 16, 0, 0);
}
__device__ __forceinline__ f32x4 mfma(bf16x8 a, bf16x8 b, f32x4 c) {
  return __builtin_amdgcn_mfma_f32_16x16x32_bf16(a, b, c, 0, 0, 0);
}
__device__ __forceinline__ bf16x8 ldsfrag(const char* p) { return *(const LDS_AS bf16x8*)p; }
__device__ __forceinline__ s16x4 ldstr(const char* p) {
  typedef short v4i16_t __attribute__((ext_vector_type(4)));
  return __builtin_bit_cast(s16x4, __builtin_amdgcn_ds_read_tr16_b64_v4i16((LDS_AS v4i16_t*)p));
}
__device__ __forceinline__ float xsum4(float x) {
  auto s = __builtin_amdgcn_permlane16_swap(__float_as_uint(x), __float_as_uint(x), false, false);
  float y = __uint_as_float(s[0]) + __uint_as_float(s[1]);
  auto t = __builtin_amdgcn_permlane32_swap(__float_as_uint(y), __float_as_uint(y), false, false);
  return __uint_as_float(t[0]) + __uint_as_float(t[1]);
}
__device__ __forceinline__ float gelu_f(float x) {
  const float c0 = -2.302208198f;
  const float c1 = -0.1029432397f;
  float e = __builtin_amdgcn_exp2f(x * (c0 + c1 * x * x));
  return x * __builtin_amdgcn_rcpf(1.0f + e);
}
__device__ __forceinline__ float phi_f(float t) {
  float e = __builtin_amdgcn_exp2f(t * 1.4426950408889634f);
  return (t > 0.f ? t + 1.0f : e) + 1e-6f;
}
#define WAITBAR(N) asm volatile("s_waitcnt vmcnt(" #N ") lgkmcnt(0)\n\ts_barrier" ::: "memory")
#define LBAR() asm volatile("s_waitcnt lgkmcnt(0)\n\ts_barrier" ::: "memory")
template <int N> struct IC { static constexpr int value = N; };
#define TS_DECL
#define TS(k)
#define TS_FLUSH(kid, base)
#define CBAR() asm volatile("" ::: "memory")
template <int N> __device__ __forceinline__ void interleave_mfma_ds() {
#pragma unroll
  for (int k = 0; k < N; k++) { __builtin_amdgcn_sched_group_barrier(0x008, 1, 0); __builtin_amdgcn_sched_group_barrier(0x100, 1, 0); }
}

template <int I, int N, class F> __device__ __forceinline__ void sfor(F&& f) {
  if constexpr (I < N) { f(IC<I>{}); sfor<I + 1, N>(f); }
}
typedef __attribute__((ext_vector_type(2))) __bf16 bf16x2;
__device__ __forceinline__ float dot2(unsigned a, unsigned b, float c) {
  return __builtin_amdgcn_fdot2_f32_bf16(__builtin_bit_cast(bf16x2, a), __builtin_bit_cast(bf16x2, b), c, false);
}
__device__ __forceinline__ void glds_sc1(const char* sbase, unsigned voff, unsigned ldsoff) {
  asm volatile("s_mov_b32 m0, %2\n\tglobal_load_lds_dwordx4 %0, %1 sc1" ::"v"(voff), "s"(sbase), "s"(ldsoff) : "memory");
}
__device__ __forceinline__ void glds_pl(const char* sbase, unsigned voff, unsigned ldsoff) {
  asm volatile("s_mov_b32 m0, %2\n\tglobal_load_lds_dwordx4 %0, %1" ::"v"(voff), "s"(sbase), "s"(ldsoff) : "memory");
}
template <int N> __device__ __forceinline__ void waitvm() { asm volatile("s_waitcnt vmcnt(%0)" ::"n"(N) : "memory"); }
template <int N> __device__ __forceinline__ void waitbar() { asm volatile("s_waitcnt vmcnt(%0) lgkmcnt(0)\n\ts_barrier" ::"n"(N) : "memory"); }
#define SBAR() asm volatile("s_barrier" ::: "memory")

constexpr int PT_UNITS = 890;
constexpr int CSP3_KS = 8192, CSP3_CB = 256;
constexpr unsigned WS_DUM = 28u << 20;
struct PTS { const char* src; unsigned dst, csp; int ld, ks, cg, kind; };
__device__ __forceinline__ PTS pt_src(int u, const bf16_t* wo, const bf16_t* w1, const bf16_t* w2, const bf16_t* w3, const bf16_t* w4) {
  PTS s; int up, col0;
  s.ld = D_; s.kind = 0; s.csp = WS_DUM + u * 512; s.cg = 0;
  if (u < 570) {
    int nks = 20;
    if (u < 100) { s.src = (const char*)w1; s.dst = WS_W1T; up = u; }
    else if (u < 200) { s.src = (const char*)w2; s.dst = WS_W2T; up = u - 100; }
    else if (u < 300) { s.src = (const char*)wo; s.dst = WS_WOT; up = u - 200; }
    else { s.src = (const char*)w4; s.dst = WS_W4T; nks = 54; up = u - 300; }
    s.ks = up / 5; s.cg = up - s.ks * 5;
    col0 = s.cg * 128;
    if (u < 100) s.csp = WS_CSP1 + (s.ks * D_ + col0) * 4;
    s.dst += s.ks * 5120;
    s.cg |= nks << 8;
  } else {
    up = u - 570;
    s.ks = up >> 4; const int cb = (up >> 1) & 7; s.kind = 1 + (up & 1);
    s.src = (const char*)w3; s.ld = F_;
    col0 = cb * 216 + (up & 1) * 128;
    s.csp = WS_CSP3 + (s.ks * 8 + cb) * 1024 + (up & 1) * 512;
    s.dst = WS_W3T + (cb * 20 + s.ks) * 14336 + (up & 1) * 8192;
  }
  s.src += ((size_t)s.ks * 32 * s.ld + col0) * 2;
  return s;
}
#define STM_P 16
#define STM_Q 16

constexpr int K1_LDS = 5 * 32768;
#define K1_PF 1
#define K1_ORD 0
#define K1_PTNT 1
__device__ __forceinline__ void glds_nt(const char* sbase, unsigned voff, unsigned ldsoff) {
  asm volatile("s_mov_b32 m0, %2\n\tglobal_load_lds_dwordx4 %0, %1 nt" ::"v"(voff), "s"(sbase), "s"(ldsoff) : "memory");
}
#define K1_PT 1
__global__ __launch_bounds__(512, 1) void k_qkv(const bf16_t* __restrict__ x, const bf16_t* __restrict__ wqkv, const bf16_t* __restrict__ wo,
                                                const bf16_t* __restrict__ w1, const bf16_t* __restrict__ w2,
                                                const bf16_t* __restrict__ w3, const bf16_t* __restrict__ w4, char* __restrict__ ws) {
  extern __shared__ __attribute__((aligned(16))) char lds[];
  const int tid = threadIdx.x, lane = tid & 63;
  const int w = __builtin_amdgcn_readfirstlane(tid >> 6);
  const int xcd = blockIdx.x & 7, idx = blockIdx.x >> 3;
  const int rt = (xcd >> 1) * 4 + (idx >> 3), hp = (xcd & 1) * 8 + (idx & 7);
  const int i = lane & 15, g = lane >> 4, r = w & 3;
  const __amdgpu_buffer_rsrc_t rs = __builtin_amdgcn_make_buffer_rsrc(ws, 0, 0x10000000, 0x00020000);
  TS_DECL;
  TS(0);
  const unsigned voffA = (unsigned)((rt * 128 + 16 * w + (lane >> 3)) * 1280 + (((lane & 7) ^ (lane >> 3)) << 4));
  unsigned voffW;
  bool wld;
  {
    const int c = (lane & 15) ^ ((lane >> 4) << 1) ^ ((w & 1) << 3);
    const int col = c < 5 ? 8 * c : c <= 10 ? 640 + 8 * (c - 6) : c == 11 ? 1280 + 32 : 1280 + 8 * (c - 12);
    voffW = (unsigned)((8 * w + (lane >> 4)) * 3840 + (40 * hp + col) * 2);
    wld = c != 5;
  }
  auto G = [&](auto SI) {
    constexpr int st = decltype(SI)::value, sl = (st & 3) * 32768;
    glds_pl((const char*)x + st * 128, voffA, sl + w * 2048);
    glds_pl((const char*)x + st * 128 + 8 * 1280, voffA, sl + w * 2048 + 1024);
    if (wld) {
      glds_pl((const char*)wqkv + st * 64 * 3840, voffW, sl + 16384 + w * 2048);
      glds_pl((const char*)wqkv + st * 64 * 3840 + 4 * 3840, voffW, sl + 16384 + w * 2048 + 1024);
    }
  };
  unsigned pfd = 0;
  if (K1_PF) {
    const int l2 = w * 18 + lane - 46;
    const int st = lane < 46 ? w : l2 < 46 ? 8 : 9;
    const int rem = idx * 46 + (lane < 46 ? lane : l2 < 46 ? l2 : l2 < 92 ? l2 - 46 : 45);
    const int r2 = rem - 512, row = r2 / 15, l = r2 - row * 15;
    const char* p = rem < 512 ? (const char*)x + (size_t)((xcd >> 1) * 512 + rem) * 1280 + st * 128
                              : (const char*)wqkv + (size_t)(st * 64 + row) * 3840 + (l / 5) * 1280 + (xcd & 1) * 640 + (l % 5) * 128;
    asm volatile("global_load_dword %0, %1, off" : "=v"(pfd) : "v"(p) : "memory");
  }
  const bool hasu = K1_PT && w < 4 && blockIdx.x * 4 + r < PT_UNITS;
  PTS ps;
  auto ptload = [&]() {
    if (hasu) {
      ps = pt_src(blockIdx.x * 4 + r, wo, w1, w2, w3, w4);
      int c0 = i ^ (g << 1), c1 = c0 ^ 8;
      if (ps.kind == 2) { c0 = c0 < 10 ? c0 : 10; c1 = c1 < 10 ? c1 : 10; }
      const unsigned v0 = (unsigned)(g * ps.ld * 2 + c0 * 16), v1 = (unsigned)(g * ps.ld * 2 + c1 * 16);
#pragma unroll
      for (int j = 0; j < 8; j++) {
        if (K1_PTNT) glds_nt(ps.src + (size_t)j * 8 * ps.ld, ((j >> 1) & 1) ? v1 : v0, 131072 + r * 8192 + j * 1024);
        else glds_pl(ps.src + (size_t)j * 8 * ps.ld, ((j >> 1) & 1) ? v1 : v0, 131072 + r * 8192 + j * 1024);
      }
    }
  };
  if (K1_ORD == 0) { ptload(); sfor<0, 4>(G); }
  else { G(IC<0>{}); ptload(); sfor<1, 4>(G); }
  if (!wld) {
    const u32x4 one = {0x3F803F80u, 0x3F803F80u, 0x3F803F80u, 0x3F803F80u};
#pragma unroll
    for (int k = 0; k < 8; k++) *(LDS_AS u32x4*)(lds + (k >> 1) * 32768 + 16384 + w * 2048 + (k & 1) * 1024 + lane * 16) = one;
  }
  int aoff[2];
#pragma unroll
  for (int m = 0; m < 2; m++) aoff[m] = ((2 * r + m) * 16 + i) * 128 + ((g ^ (i & 7)) << 4);
  float* tmean = (float*)(lds + 65536);
  float* trstd = tmean + 128;
  float* tcs = tmean + 256;
  char* red = lds + 65536 + 4096;
  const bf16x8 ones = {0x3F80, 0x3F80, 0x3F80, 0x3F80, 0x3F80, 0x3F80, 0x3F80, 0x3F80};
  const f32x4 z4 = {0, 0, 0, 0};

  auto role = [&](auto ISQ_, auto HASU_) {
    constexpr bool ISQ = decltype(ISQ_)::value, HASU = decltype(HASU_)::value;
    constexpr int NT = ISQ ? 3 : 5, NT0 = ISQ ? 0 : 3, PS = HASU ? 10 : 0;
    int woff[NT];
#pragma unroll
    for (int u = 0; u < NT; u++) {
      const int nt = NT0 + u, q = i >> 2, p = i & 3, f = q | ((g & 1) << 2);
      woff[u] = 16384 + (8 * g + q) * 256 + (((nt * 2 + (p >> 1)) ^ (f << 1)) << 4) + (p & 1) * 8;
    }
    f32x4 acc[2][NT], cs[NT], gr[2] = {z4, z4};
#pragma unroll
    for (int u = 0; u < NT; u++) { acc[0][u] = z4; acc[1][u] = z4; cs[u] = z4; }
    struct Frag { bf16x8 a[2], w[NT]; };
    auto rd = [&](Frag& f, int slot, int kk) {
      const char* sl = lds + slot * 32768;
#pragma unroll
      for (int m = 0; m < 2; m++) f.a[m] = ldsfrag(sl + (aoff[m] ^ (kk * 64)));
#pragma unroll
      for (int u = 0; u < NT; u++) {
        s16x4 lo = ldstr(sl + woff[u] + kk * 8192), hi = ldstr(sl + woff[u] + kk * 8192 + 1024);
        f.w[u] = __builtin_shufflevector(lo, hi, 0, 1, 2, 3, 4, 5, 6, 7);
      }
    };
    auto mm = [&](const Frag& f) {
#pragma unroll
      for (int u = 0; u < NT; u++)
#pragma unroll
        for (int m = 0; m < 2; m++) acc[m][u] = ISQ ? mfma(f.w[u], f.a[m], acc[m][u]) : mfma(f.a[m], f.w[u], acc[m][u]);
      if constexpr (ISQ) { gr[0] = mfma(f.a[0], f.a[0], gr[0]); gr[1] = mfma(f.a[1], f.a[1], gr[1]); }
    };
    auto mcs = [&](const Frag& f, int sel) {
      if (r == sel) {
#pragma unroll
        for (int u = 0; u < NT; u++) cs[u] = ISQ ? mfma(f.w[u], ones, cs[u]) : mfma(ones, f.w[u], cs[u]);
      }
    };
    auto ptile = [&]() {
      const int q = i >> 2, p = i & 3, f = q | ((g & 1) << 2), pp = 2 * (p & 1) + (p >> 1);
      const char* sb = lds + 131072 + r * 8192 + (8 * g + q) * 256;
      const int nb = (ps.cg >> 8) * 5120, cg8 = (ps.cg & 255) * 8;
      float cv[8];
#pragma unroll
      for (int j = 0; j < 8; j++) {
        int jj = j; bool n32; unsigned dst;
        if (ps.kind == 0) { const int Gi = cg8 + j, cb = Gi / 5, r5 = Gi - cb * 5; n32 = r5 < 4; dst = cb * nb + (n32 ? (r5 >> 1) * 2048 + (r5 & 1) * 256 : 4096); }
        else if (ps.kind == 1) { n32 = true; dst = (j >> 1) * 2048 + (j & 1) * 256; }
        else { n32 = false; jj = j < 5 ? j : 5; dst = jj * 1024; }
        const int pe = n32 ? pp : p;
        const char* a = sb + (((jj * 2 + (pe >> 1)) ^ (f << 1)) << 4) + (pe & 1) * 8;
        s16x4 lo = ldstr(a), hi = ldstr(a + 1024);
        bf16x8 v = __builtin_shufflevector(lo, hi, 0, 1, 2, 3, 4, 5, 6, 7);
        const unsigned vo = n32 ? (unsigned)((g >> 1) * 1024 + (32 * (g & 1) + i) * 16) : (unsigned)(lane * 16);
        __builtin_amdgcn_raw_buffer_store_b128(__builtin_bit_cast(u32x4, v), rs, vo, ps.dst + dst, STM_P);
        cv[j] = mfma(ones, v, z4)[0];
      }
      const bool n32a = ps.kind == 1;
#pragma unroll
      for (int h = 0; h < 2; h++) {
        const float s = g == 0 ? cv[4 * h] : g == 1 ? cv[4 * h + 1] : g == 2 ? cv[4 * h + 2] : cv[4 * h + 3];
        int Gj = 4 * h + g;
        bool n32 = n32a;
        if (ps.kind == 0) { const int Gi = cg8 + Gj; n32 = Gi - (Gi / 5) * 5 < 4; }
        const int ci = n32 ? 8 * ((i >> 2) & 1) + 4 * (i >> 3) + (i & 3) : i;
        int col = Gj * 16 + ci;
        if (ps.kind == 2 && col >= 88) col = 87 + 0 * col;
        const bool ok = !(ps.kind == 2 && Gj * 16 + ci >= 88);
        __builtin_amdgcn_raw_buffer_store_b32(__float_as_uint(s), rs, ok ? ps.csp + (unsigned)col * 4 : WS_DUM + (blockIdx.x * 4 + r) * 512 + lane * 4, 0, STM_P);
      }
    };
    Frag f0, f1;
    if constexpr (HASU) {
      waitvm<K1_ORD == 0 ? 16 : 12>();
      ptile();
      __builtin_amdgcn_sched_barrier(0);
    }
    waitbar<12 + PS>();
    asm volatile("" ::"v"(pfd));
    TS(1);
    rd(f0, 0, 0);
    sfor<0, 10>([&](auto SI) {
      constexpr int s = decltype(SI)::value;
      rd(f1, s & 3, 1);
      mm(f0);
      interleave_mfma_ds<2 * NT>();
      __builtin_amdgcn_sched_barrier(0);
      mcs(f0, (2 * s) & 3);
      __builtin_amdgcn_sched_barrier(0);
      if constexpr (s < 9) {
        constexpr int b = s + 1;
        constexpr int yo = b <= 3 ? 8 + PS : b <= 7 ? 8 : b == 8 ? 4 : 0;
        waitbar<yo>();
        if constexpr (b + 3 < 10) G(IC<b + 3>{});
        rd(f0, b & 3, 0);
      }
      mm(f1);
      if constexpr (s < 9) interleave_mfma_ds<2 * NT>();
      __builtin_amdgcn_sched_barrier(0);
      mcs(f1, (2 * s + 1) & 3);
      __builtin_amdgcn_sched_barrier(0);
    });
    TS(2);
    if constexpr (ISQ) {
#pragma unroll
      for (int m = 0; m < 2; m++) {
        auto sw = __builtin_amdgcn_permlane32_swap(__float_as_uint(acc[m][2][0]), __float_as_uint(acc[m][2][0]), false, false);
        const float mean = __uint_as_float(sw[1]) * (1.0f / D_);
        const float q = (i & 3) == 0 ? gr[m][0] : (i & 3) == 1 ? gr[m][1] : (i & 3) == 2 ? gr[m][2] : gr[m][3];
        const float rstd = __builtin_amdgcn_rsqf(fmaxf(q * (1.0f / D_) - mean * mean, 0.f) + 1e-5f);
        if ((i >> 2) == g) { tmean[32 * r + 16 * m + i] = mean; trstd[32 * r + 16 * m + i] = rstd; }
      }
      if (i == 0) {
#pragma unroll
        for (int u = 0; u < 3; u++) *(f32x4*)(tcs + r * 48 + u * 16 + 4 * g) = cs[u];
      }
      LBAR();
      TS(3);
      f32x4 c4[3];
      float mean[2], rstd[2];
#pragma unroll
      for (int m = 0; m < 2; m++) { mean[m] = tmean[32 * r + 16 * m + i]; rstd[m] = trstd[32 * r + 16 * m + i]; }
#pragma unroll
      for (int u = 0; u < 3; u++) {
        const float* p = tcs + u * 16 + 4 * g;
        c4[u] = (*(const f32x4*)p + *(const f32x4*)(p + 48)) + (*(const f32x4*)(p + 96) + *(const f32x4*)(p + 144));
      }
      unsigned pk[2][3][2];
#pragma unroll
      for (int m = 0; m < 2; m++)
#pragma unroll
        for (int u = 0; u < 3; u++) {
          float v[4];
#pragma unroll
          for (int e = 0; e < 4; e++) v[e] = phi_f(rstd[m] * (acc[m][u][e] - mean[m] * c4[u][e]));
          pk[m][u][0] = cvtpk(v[0], v[1]); pk[m][u][1] = cvtpk(v[2], v[3]);
        }
      const unsigned qb = (unsigned)(((rt * 2 + (r >> 1)) * 8 + (hp >> 1)) * 10240 + (hp & 1) * 5120 + ((r & 1) * 32 + i) * 16);
#pragma unroll
      for (int m = 0; m < 2; m++) {
        auto x0 = __builtin_amdgcn_permlane16_swap(pk[m][0][0], pk[m][1][0], false, false);
        auto x1 = __builtin_amdgcn_permlane16_swap(pk[m][0][1], pk[m][1][1], false, false);
        __builtin_amdgcn_raw_buffer_store_b128(u32x4{x0[0], x1[0], x0[1], x1[1]}, rs, qb + m * 256 + (2 * (g & 1) + (g >> 1)) * 1024, WS_PQ, STM_Q);
      }
      {
        auto x0 = __builtin_amdgcn_permlane16_swap(pk[0][2][0], pk[1][2][0], false, false);
        auto x1 = __builtin_amdgcn_permlane16_swap(pk[0][2][1], pk[1][2][1], false, false);
        if (g < 2) __builtin_amdgcn_raw_buffer_store_b128(u32x4{x0[0], x1[0], x0[1], x1[1]}, rs, qb + (g & 1) * 256 + 4 * 1024, WS_PQ, STM_Q);
      }
      {
        const int t = (blockIdx.x * 4 + r) * 64 + lane;
        if (t < FL_BYTES / 16) __builtin_amdgcn_raw_buffer_store_b128(u32x4{0, 0, 0, 0}, rs, t * 16, WS_FL, STM_Q);
        else if (t < FL_BYTES / 16 + 320) {
          const int z = t - FL_BYTES / 16, q = z >> 2, k = z & 3;
          const unsigned o = k < 2 ? frag_off(q, 62 + k) : 33u * T640 + frag_off(q, k - 2);
          __builtin_amdgcn_raw_buffer_store_b128(u32x4{0, 0, 0, 0}, rs, o, WS_H1G, STM_Q);
        }
      }
      LBAR();
    } else {
      if (g == 0) {
#pragma unroll
        for (int u = 0; u < 5; u++) tcs[192 + r * 80 + u * 16 + i] = cs[u][0];
      }
      LBAR();
      TS(3);
      bf16x8 op[5];
      f32x4 m4[2], r4[2];
#pragma unroll
      for (int m = 0; m < 2; m++) { m4[m] = *(const f32x4*)(tmean + 32 * r + 16 * m + 4 * g); r4[m] = *(const f32x4*)(trstd + 32 * r + 16 * m + 4 * g); }
#pragma unroll
      for (int u = 0; u < 5; u++) {
        const float* p = tcs + 192 + u * 16 + i;
        const float c = (p[0] + p[80]) + (p[160] + p[240]);
        u32x4 o;
#pragma unroll
        for (int m = 0; m < 2; m++) {
          float v[4];
#pragma unroll
          for (int e = 0; e < 4; e++) {
            float a = r4[m][e] * (acc[m][u][e] - m4[m][e] * c);
            v[e] = u < 2 ? phi_f(a) : u == 2 ? (i < 8 ? phi_f(a) : a) : a;
          }
          o[2 * m] = cvtpk(v[0], v[1]); o[2 * m + 1] = cvtpk(v[2], v[3]);
        }
        op[u] = __builtin_bit_cast(bf16x8, o);
      }
      char* rw = red + r * 10240 + lane * 16;
      *(f32x4*)(rw + 0 * 1024) = mfma(op[0], op[3], z4);
      *(f32x4*)(rw + 1 * 1024) = mfma(op[0], op[4], z4);
      *(f32x4*)(rw + 2 * 1024) = mfma(op[1], op[3], z4);
      *(f32x4*)(rw + 3 * 1024) = mfma(op[1], op[4], z4);
      *(f32x4*)(rw + 4 * 1024) = mfma(op[1], op[2], z4);
      *(f32x4*)(rw + 5 * 1024) = mfma(op[2], op[4], z4);
      *(f32x4*)(rw + 6 * 1024) = mfma(op[2], op[2], z4);
      *(f32x4*)(rw + 7 * 1024) = mfma(op[0], ones, z4);
      *(f32x4*)(rw + 8 * 1024) = mfma(op[1], ones, z4);
      *(f32x4*)(rw + 9 * 1024) = mfma(op[2], ones, z4);
      LBAR();
      const int t = tid - 256;
      if (t < 210) {
        const int h = t >= 105, rem = t - 105 * h;
        const bool isz = rem >= 100;
        const int e = isz ? 0 : rem / 5, dq = isz ? rem - 100 : rem - e * 5;
        int dt, gg, et, ii;
        if (h == 0) { dt = dq < 4 ? 0 : 1; gg = dq < 4 ? dq : 0; et = e < 16 ? 0 : 1; ii = e & 15; }
        else { dt = dq < 3 ? 1 : 2; gg = dq < 3 ? dq + 1 : dq - 3; et = e < 12 ? 1 : 2; ii = e < 12 ? e + 4 : e - 4; }
        const int tile = isz ? 7 + dt : dt == 0 ? et : dt == 1 ? 2 + et : 4 + et;
        const char* p = red + tile * 1024 + (gg * 16 + (isz ? 0 : ii)) * 16;
        f32x4 s = *(const f32x4*)p;
#pragma unroll
        for (int k = 1; k < 4; k++) s += *(const f32x4*)(p + k * 10240);
        __builtin_amdgcn_raw_buffer_store_b128(__builtin_bit_cast(u32x4, s), rs, (unsigned)(((rt * 16 + hp) * 210 + t) * 16), WS_REC, STM_Q);
      }
    }
    TS(5);
  };
  if (w >= 4) role(IC<0>{}, IC<0>{});
  else if (hasu) role(IC<1>{}, IC<K1_PT>{});
  else role(IC<1>{}, IC<0>{});
  if (w == 0) TS_FLUSH(1, 0);
  if (w == 4) TS_FLUSH(1, 8);
}

typedef __attribute__((ext_vector_type(16))) float f32x16;
typedef GLB_AS unsigned gu32;
constexpr int LX_M = 131072, LX_A = 147456, L_ST = 159744, L_CS = 160768, L_T = 131072, CH_LDS = 162048;
#define CH_RD 28
#define CH_QZ 4
constexpr int RD = CH_RD, NFR = 154, QZ = CH_QZ;
constexpr int PBND[6] = {0, 20, 40, 60, 100, 154};
#define CH_NPRE 4
#define CH_SL 1
#define CH_DEN 2
#define CH_T2 88
constexpr int NPRE = CH_NPRE;
struct LSched { int step[NFR]; int pre[5]; int pub[5]; int pre2, pub2; };
constexpr LSched make_sched() {
  LSched s{};
  s.pre2 = 20 + NPRE; s.pub2 = RD < 40 ? RD : 40;
  int issued = s.pub2;
  constexpr int TG[5] = {40, 60, CH_T2, 128, 154};
  for (int p = 0; p < 5; p++) {
    for (int f = PBND[p]; f < PBND[p + 1]; f++) {
      const int den = p < 3 ? CH_DEN : 1;
      bool go = (f - PBND[p]) % den == 0 && issued < TG[p] && issued < f + 1 + RD;
      if (issued >= PBND[p + 1] && f >= PBND[p + 1] - QZ) go = false;
      if (issued < PBND[p + 1] && issued <= f + 4) go = true;
      if (go) issued++;
      s.step[f] = issued;
    }
    int lim = PBND[p + 1] + RD; if (lim > TG[p]) lim = TG[p];
    if (lim < issued) lim = issued;
    s.pre[p] = issued + NPRE < lim ? issued + NPRE : lim;
    issued = lim;
    s.pub[p] = issued;
  }
  return s;
}
constexpr LSched LS = make_sched();
constexpr int MON = 1;
constexpr int GON = 1;

__device__ __forceinline__ f32x16 mfma32(bf16x8 a, bf16x8 b, f32x16 c) {
  return __builtin_amdgcn_mfma_f32_32x32x16_bf16(a, b, c, 0, 0, 0);
}
template <int N> __device__ __forceinline__ void waitvmc() { __builtin_amdgcn_s_waitcnt((N & 15) | ((N >> 4) << 14) | 0x0F70); }
__device__ __forceinline__ void glds_pf(const char* sbase, unsigned voff, unsigned ldsoff) {
  asm volatile("s_mov_b32 m0, %2\n\tglobal_load_lds_dword %0, %1" ::"v"(voff), "s"(sbase), "s"(ldsoff) : "memory");
}
template <int N> __device__ __forceinline__ void waitbarv() { asm volatile("s_waitcnt vmcnt(%0)\n\ts_barrier" ::"n"(N) : "memory"); }
#define VM0() __builtin_amdgcn_s_waitcnt(0x0F70)
#define CTS_DECL
#define CTS(k)
#define CTS_FLUSH(base)

__global__ __launch_bounds__(512, 1) void k_chain(const bf16_t* __restrict__ x, char* __restrict__ ws, bf16_t* __restrict__ outp) {
  extern __shared__ __attribute__((aligned(16))) char lds[];
  const int tid = threadIdx.x, lane = tid & 63;
  const int w = __builtin_amdgcn_readfirstlane(tid >> 6);
  const int xcd = blockIdx.x & 7, idx = blockIdx.x >> 3;
  const int rt = xcd * 4 + (idx >> 3), cb = idx & 7;
  const __amdgpu_buffer_rsrc_t rs = __builtin_amdgcn_make_buffer_rsrc(ws, 0, 0x10000000, 0x00020000);
  auto bload = [&](unsigned v, unsigned so) { return __builtin_bit_cast(bf16x8, __builtin_amdgcn_raw_buffer_load_b128(rs, v, so, 0)); };
  auto bst16 = [&](unsigned v, unsigned so, u32x4 d) { __builtin_amdgcn_raw_buffer_store_b128(d, rs, v, so, 16); };
  auto bst8 = [&](unsigned v, unsigned so, u32x2 d) { __builtin_amdgcn_raw_buffer_store_b64(d, rs, v, so, 16); };
  CTS_DECL;
  CTS(0);

  f32x4 rv[20];
  bf16x8 bq[3];
  int rn = 16;
  {
    const int hpl = tid >= 210;
    unsigned rb = (unsigned)WS_REC + (unsigned)(((2 * cb + hpl) * 210 + tid - 210 * hpl) * 16), rst = 53760;
    if (tid >= 420) {
      rn = tid < 440 || (tid >= 448 && tid < 502) ? 20 : 0;
      rst = rn == 0 ? 0 : tid < 448 ? 2560 : CSP3_KS;
      rb = rn == 0 ? (unsigned)WS_REC : tid < 448 ? (unsigned)WS_CSP1 + (cb * 80 + 4 * (tid - 420)) * 4 : (unsigned)WS_CSP3 + (cb * CSP3_CB + 4 * (tid - 448)) * 4;
    }
#pragma unroll
    for (int p = 0; p < 16; p++) rv[p] = __builtin_bit_cast(f32x4, __builtin_amdgcn_raw_buffer_load_b128(rs, rb + p * rst, 0, 0));
    if (w >= 6) {
#pragma unroll
      for (int p = 16; p < 20; p++) rv[p] = __builtin_bit_cast(f32x4, __builtin_amdgcn_raw_buffer_load_b128(rs, rb + (rn == 20 ? p * rst : 0), 0, 0));
    }
    const int g = lane >> 4;
#pragma unroll
    for (int s = 0; s < 3; s++) {
      const int c = 4 * s + g < 9 ? 4 * s + g : 9;
      bq[s] = bload((unsigned)((rt * 8 + cb) * 10240 + c * 1024 + (16 * (w & 3) + (lane & 15)) * 16), WS_PQ);
    }
    CBAR();
#pragma unroll
    for (int k = 0; k < 3; k++)
      if (tid + 512 * k < 1092) *(LDS_AS u32x4*)(lds + L_T + (tid + 512 * k) * 16) = u32x4{0, 0, 0, 0};
  }
  auto p2 = [&]() {
    const int i = lane & 15, g = lane >> 4;
    LBAR();
    f32x4 s = ((rv[0] + rv[1]) + (rv[2] + rv[3])) + ((rv[4] + rv[5]) + (rv[6] + rv[7])) +
              (((rv[8] + rv[9]) + (rv[10] + rv[11])) + ((rv[12] + rv[13]) + (rv[14] + rv[15])));
    if (w >= 6) { if (rn == 20) s += (rv[16] + rv[17]) + (rv[18] + rv[19]); }
    CTS(24);
    if (tid < 420) {
      const int hpl = tid >= 210, t = tid - 210 * hpl, h = t >= 105, rem = t - 105 * h, hl = 2 * hpl + h;
      const int e = rem / 5, dq = rem - e * 5;
      const int n = e == 20 ? 80 + hl : hl * 20 + e;
      *(LDS_AS u32x2*)(lds + L_T + n * 208 + (hl * 20 + 4 * dq) * 2) = u32x2{cvtpk(s[0], s[1]), cvtpk(s[2], s[3])};
    } else if (rn == 20) {
      *(LDS_AS f32x4*)(lds + L_CS + (tid < 448 ? (tid - 420) * 16 : 320 + (tid - 448) * 16)) = s;
    }
    LBAR();
    const char* tp = lds + L_T + i * 208 + g * 16;
    const char* zp = lds + L_T + (80 + (i & 3)) * 208 + g * 16;
    const f32x4 z4 = {0, 0, 0, 0};
    f32x4 dn = mfma(ldsfrag(zp), bq[0], z4);
    dn = mfma(ldsfrag(zp + 64), bq[1], dn);
    dn = mfma(ldsfrag(zp + 128), bq[2], dn);
    const unsigned so = (unsigned)(rt * T640) + frag_off(cb * 10 + (g >> 1), 16 * (w & 3) + i) + (g & 1) * 8;
    auto fin = [&](auto UI, const f32x4& nu) {
      constexpr int u = decltype(UI)::value;
      const float d = u == 0 ? dn[0] : u == 1 ? (g == 0 ? dn[0] : dn[1]) : u == 2 ? (g < 2 ? dn[1] : dn[2]) : u == 3 ? (g < 3 ? dn[2] : dn[3]) : dn[3];
      const float inv = __builtin_amdgcn_rcpf(d + 1e-6f);
      const unsigned o = (unsigned)(rt * T640) + frag_off(cb * 10 + 2 * u + (g >> 1), 16 * (w & 3) + i) + (g & 1) * 8;
      bst8(o, WS_O, u32x2{cvtpk(nu[0] * inv, nu[1] * inv), cvtpk(nu[2] * inv, nu[3] * inv)});
    };
    (void)so;
    if (w < 4) {
      f32x4 n0 = mfma(ldsfrag(tp), bq[0], z4);
      f32x4 n1 = mfma(ldsfrag(tp + 16 * 208), bq[0], z4);
      f32x4 n2 = mfma(ldsfrag(tp + 32 * 208), bq[0], z4);
      n1 = mfma(ldsfrag(tp + 16 * 208 + 64), bq[1], n1);
      n2 = mfma(ldsfrag(tp + 32 * 208 + 64), bq[1], n2);
      fin(IC<0>{}, n0); fin(IC<1>{}, n1); fin(IC<2>{}, n2);
    } else {
      f32x4 n3 = mfma(ldsfrag(tp + 48 * 208 + 64), bq[1], z4);
      f32x4 n4 = mfma(ldsfrag(tp + 64 * 208 + 64), bq[1], z4);
      n3 = mfma(ldsfrag(tp + 48 * 208 + 128), bq[2], n3);
      n4 = mfma(ldsfrag(tp + 64 * 208 + 128), bq[2], n4);
      fin(IC<3>{}, n3); fin(IC<4>{}, n4);
    }
    CTS(25);
  };

  if (w < 4) {
    const int t = w & 1, kh = w >> 1, hh = lane >> 5, l31 = lane & 31;
    const unsigned vA = cb * 102400 + kh * 5120 + t * 2048 + lane * 16;
    const unsigned vB = cb * 286720 + w * 2048 + lane * 16;
    const unsigned vC = cb * 276480 + kh * 5120 + t * 2048 + lane * 16;
    bf16x8 ring[RD];
    auto wl = [&](auto FI) {
      constexpr int F = decltype(FI)::value;
      constexpr int ph = F < 20 ? 0 : F < 40 ? 1 : F < 60 ? 2 : F < 100 ? 3 : 4;
      constexpr int f = F - PBND[ph];
      constexpr unsigned so = ph == 0 ? WS_WOT + (f >> 1) * 10240 : ph == 1 ? WS_W1T + (f >> 1) * 10240 : ph == 2 ? WS_W2T + (f >> 1) * 10240
                            : ph == 3 ? WS_W3T + (f >> 1) * 14336 : WS_W4T + (f >> 1) * 10240;
      ring[F % RD] = bload((ph < 3 ? vA : ph == 3 ? vB : vC) + (f & 1) * 1024, so);
    };
    sfor<0, 20>(wl);
    const int row = 32 * kh + l31;
    const int colq = cb * 10 + 4 * t + hh;
    u32x4 hr[2], x1r[2];
    {
      const bf16_t* xp = x + (size_t)(rt * 64 + row) * D_ + colq * 8;
      hr[0] = *(const u32x4*)xp; hr[1] = *(const u32x4*)(xp + 16);
    }
    const unsigned so640[2] = {(unsigned)(rt * T640) + frag_off(colq, row), (unsigned)(rt * T640) + frag_off(colq + 2, row)};
    const unsigned lp = ((lane >> 4) & 1) * 1024 + hh * 256 + (lane & 15) * 16;
    const char* aA[2] = {lds + kh * 4096 + kh * 2048 + lp, lds + kh * 4096 + (kh ^ 1) * 2048 + lp};
    const char* aL = lds + lp;
    f32x16 acc[2];
    bf16x8 af[3][2];
    float mean1 = 0.f, r1 = 0.f;

    auto gemm = [&](auto PH) {
      constexpr int ph = decltype(PH)::value;
      constexpr int F0 = PBND[ph], NS = PBND[ph + 1] - F0;
      auto rdA = [&](auto SI) {
        constexpr int s = decltype(SI)::value;
        if constexpr (ph == 3) {
          constexpr int o = (s >> 1) * 4096 + (s & 1) * 512;
          af[s % 3][0] = ldsfrag(aL + o); af[s % 3][1] = ldsfrag(aL + o + 2048);
        } else {
          constexpr int o = (ph == 4 ? ((s >> 2) & 7) * 16384 + ((s >> 1) & 1) * 8192 : (s >> 1) * 8192) + (s & 1) * 512;
          af[s % 3][0] = ldsfrag(aA[0] + o); af[s % 3][1] = ldsfrag(aA[1] + o);
        }
      };
#pragma unroll
      for (int e = 0; e < 16; e++) { acc[0][e] = 0.f; acc[1][e] = 0.f; }
      SBAR();
      CTS(1 + 4 * ph);
      rdA(IC<0>{}); rdA(IC<1>{});
      __builtin_amdgcn_sched_barrier(0);
      sfor<0, NS>([&](auto SI) {
        constexpr int s = decltype(SI)::value;
        if constexpr (s + 2 < NS) {
          if constexpr (((s + 2) & (ph == 3 ? 7 : 3)) == 0) SBAR();
          rdA(IC<s + 2>{});
        }
        acc[0] = mfma32(ring[(F0 + s) % RD], af[s % 3][0], acc[0]);
        acc[1] = mfma32(ring[(F0 + s) % RD], af[s % 3][1], acc[1]);
        constexpr int l0 = (F0 + s) == 0 ? LS.pub2 : (s == 0 ? LS.pub[ph > 0 ? ph - 1 : 0] : LS.step[F0 + s - (F0 + s > 0 ? 1 : 0)]);
        sfor<l0, LS.step[F0 + s]>(wl);
        __builtin_amdgcn_sched_barrier(0);
      });
      CTS(2 + 4 * ph);
    };
    auto exch = [&]() {
      char* xw = lds + LX_M + w * 4096 + lane * 16;
      const char* xr = lds + LX_M + (w ^ 2) * 4096 + lane * 16;
#pragma unroll
      for (int j = 0; j < 4; j++) *(LDS_AS f32x4*)(xw + j * 1024) = f32x4{acc[1][4 * j], acc[1][4 * j + 1], acc[1][4 * j + 2], acc[1][4 * j + 3]};
      LBAR();
#pragma unroll
      for (int j = 0; j < 4; j++) {
        f32x4 v = *(const LDS_AS f32x4*)(xr + j * 1024);
#pragma unroll
        for (int e = 0; e < 4; e++) acc[0][4 * j + e] += v[e];
      }
    };
    auto wlp = [&](auto FI) { wl(FI); __builtin_amdgcn_s_sleep(CH_SL); __builtin_amdgcn_sched_barrier(0); };
    auto publish = [&](auto PH) {
      constexpr int ph = decltype(PH)::value;
      CTS(3 + 4 * ph);
      __builtin_amdgcn_sched_barrier(0);
      sfor<LS.step[PBND[ph + 1] - 1], LS.pre[ph]>(wlp);
      waitvmc<MON * (LS.pre[ph] - LS.step[PBND[ph + 1] - 1])>();
      SBAR();
      CTS(4 + 4 * ph);
      sfor<LS.pre[ph], LS.pub[ph]>(wlp);
      __builtin_amdgcn_sched_barrier(0);
    };
    auto res = [&](const u32x4& r, int e) { return (e & 1) ? bfhi(r[e >> 1]) : bflo(r[e >> 1]); };

    p2();
    __builtin_amdgcn_sched_barrier(0);
    sfor<20, LS.pre2>(wlp);
    waitvmc<MON * (LS.pre2 - 20)>();
    SBAR();
    CTS(26);
    sfor<LS.pre2, LS.pub2>(wlp);
    __builtin_amdgcn_sched_barrier(0);
    gemm(IC<0>{});
    exch();
#pragma unroll
    for (int c = 0; c < 2; c++) {
#pragma unroll
      for (int d = 0; d < 4; d++)
        hr[c][d] = cvtpk(res(hr[c], 2 * d) + acc[0][8 * c + 2 * d], res(hr[c], 2 * d + 1) + acc[0][8 * c + 2 * d + 1]);
      bst16(so640[c], WS_H, hr[c]);
    }
    publish(IC<0>{});
    gemm(IC<1>{});
    exch();
    {
      f32x4 st = *(const LDS_AS f32x4*)(lds + L_ST + row * 16);
      mean1 = st[0]; r1 = st[1];
      const float rs1 = st[2];
#pragma unroll
      for (int c = 0; c < 2; c++) {
        const char* cp = lds + L_CS + (32 * t + 16 * c + 8 * hh) * 4;
        f32x4 c0 = *(const LDS_AS f32x4*)cp, c1 = *(const LDS_AS f32x4*)(cp + 16);
        float v[8];
#pragma unroll
        for (int e = 0; e < 8; e++) v[e] = gelu_f(rs1 * (acc[0][8 * c + e] - mean1 * (e < 4 ? c0[e & 3] : c1[e & 3])));
        bst16(so640[c], WS_H1, u32x4{cvtpk(v[0], v[1]), cvtpk(v[2], v[3]), cvtpk(v[4], v[5]), cvtpk(v[6], v[7])});
      }
    }
    publish(IC<1>{});
    gemm(IC<2>{});
    exch();
#pragma unroll
    for (int c = 0; c < 2; c++) {
#pragma unroll
      for (int d = 0; d < 4; d++)
        x1r[c][d] = cvtpk((res(hr[c], 2 * d) - mean1) * r1 + acc[0][8 * c + 2 * d], (res(hr[c], 2 * d + 1) - mean1) * r1 + acc[0][8 * c + 2 * d + 1]);
      bst16(so640[c], WS_X1, x1r[c]);
    }
    publish(IC<2>{});
    gemm(IC<3>{});
    LBAR();
    {
      const int q0 = cb * 27 + 4 * w + hh;
#pragma unroll
      for (int c = 0; c < 2; c++) {
        const char* cp = lds + L_CS + 320 + (32 * w + 16 * c + 8 * hh) * 4;
        f32x4 c0 = *(const LDS_AS f32x4*)cp, c1 = *(const LDS_AS f32x4*)(cp + 16);
#pragma unroll
        for (int p = 0; p < 2; p++) {
          f32x4 st = *(const LDS_AS f32x4*)(lds + L_ST + (32 * p + l31) * 16);
          float v[8];
#pragma unroll
          for (int e = 0; e < 8; e++) v[e] = gelu_f(st[1] * (acc[p][8 * c + e] - st[0] * (e < 4 ? c0[e & 3] : c1[e & 3])));
          bst16((unsigned)(rt * T1728) + frag_off(q0 + 2 * c, 32 * p + l31), WS_H2,
                u32x4{cvtpk(v[0], v[1]), cvtpk(v[2], v[3]), cvtpk(v[4], v[5]), cvtpk(v[6], v[7])});
        }
      }
    }
    publish(IC<3>{});
    gemm(IC<4>{});
    exch();
    {
      int ln = lane; asm volatile("" : "+v"(ln));
      bf16_t* op = outp + (size_t)(rt * 64 + 32 * kh + (ln & 31)) * D_ + (cb * 10 + 4 * t + (ln >> 5)) * 8;
#pragma unroll
      for (int c = 0; c < 2; c++) {
        u32x4 o;
#pragma unroll
        for (int d = 0; d < 4; d++)
          o[d] = cvtpk(res(hr[c], 2 * d) + res(x1r[c], 2 * d) + acc[0][8 * c + 2 * d],
                       res(hr[c], 2 * d + 1) + res(x1r[c], 2 * d + 1) + acc[0][8 * c + 2 * d + 1]);
        *(u32x4*)(op + 16 * c) = o;
      }
    }
    CTS(19); CTS(20); CTS(21); CTS(22); CTS(23); CTS(27);
    if (w == 0) CTS_FLUSH(0);
    return;
  }

  const int a = w - 4, i = lane & 15, g = lane >> 4;
  const int row = 16 * a + i;
  bf16x8 G[59];
  const unsigned vG = cb * 102400 + a * 5120 + 4096 + lane * 16;
  const unsigned vG7 = cb * 276480 + a * 5120 + 4096 + lane * 16;
  const int b = a & 1;
  const unsigned vE = cb * 286720 + b * 14336 + (8 + a) * 1024 + lane * 16;
  const unsigned vO = cb * 286720 + (1 - b) * 14336 + (8 + a) * 1024 + lane * 16;
  const unsigned vS = cb * 286720 + b * 14336 + (12 + (a >> 1)) * 1024 + lane * 16;
  auto gl = [&](auto GI) {
    constexpr int n = decltype(GI)::value;
    if constexpr (GON == 0) { bf16x8 tmp_; asm volatile("" : "=v"(tmp_)); G[n] = tmp_; }
    else if constexpr (n < 5) G[n] = bload(vG, WS_WOT + n * 20480);
    else if constexpr (n < 10) G[n] = bload(vG, WS_W1T + (n - 5) * 20480);
    else if constexpr (n < 15) G[n] = bload(vG, WS_W2T + (n - 10) * 20480);
    else if constexpr (n < 45) {
      constexpr int c = (n - 15) / 6, k = (n - 15) % 6;
      if constexpr (k < 4) G[n] = bload((k & 1) ? vO : vE, WS_W3T + (4 * c + (k & ~1)) * 14336);
      else G[n] = bload(vS, WS_W3T + (4 * c + 2 * (k - 4)) * 14336);
    } else G[n] = bload(vG7, WS_W4T + (n - 45) * 20480);
  };
  const unsigned vT = rt * T640 + a * 1024 + lane * 16;
#define CH_GS {{9, 11, 12, 13, 15, 18}, {20, 22, 24, 26, 28, 31}, {33, 35, 37, 39, 41, 43}, {45, 48, 51, 55, 59, 59}}
  constexpr int GS[4][6] = CH_GS;
  constexpr int G2 = 7;
  u32x2 hr, x1r;
  hr = *(const u32x2*)(x + (size_t)(rt * 64 + row) * D_ + cb * 80 + 64 + 4 * g);
  sfor<0, 5>(gl);
  __builtin_amdgcn_sched_barrier(0);
  const unsigned so640 = rt * T640 + frag_off(cb * 10 + 8 + (g >> 1), row) + (g & 1) * 8;
  float mean1 = 0.f, r1 = 0.f;
  const char* aR[4];
#pragma unroll
  for (int p = 0; p < 4; p++) aR[p] = lds + a * 4096 + (a ^ p) * 1024 + lane * 16;
  const char* aSt[3];
#pragma unroll
  for (int d = 1; d < 4; d++) aSt[d - 1] = lds + (a ^ d) * 4096 + a * 1024 + lane * 16;
  gu32* flags = (gu32*)(ws + WS_FL);
  auto poll = [&](int ph) {
    int r2 = rt, wd = lane & 7;
    bool need = lane < 8;
    if (ph == 2) {
      if (lane >= 8 && lane < 16 && rt > 0 && (wd & 2) == 0) { r2 = rt - 1; need = true; }
      if (lane >= 16 && lane < 24 && rt < 31 && (wd & 2) != 0) { r2 = rt + 1; need = true; }
    }
    gu32* p = flags + (ph * 32 + r2) * 64 + wd;
#pragma unroll 1
    for (unsigned spins = 0; spins < (1u << 18); spins++) {
      unsigned v = need ? __hip_atomic_load(p, __ATOMIC_RELAXED, __HIP_MEMORY_SCOPE_AGENT) : 1u;
      if (__all(v == 1u)) break;
      __builtin_amdgcn_s_sleep(1);
    }
  };
  auto publish = [&](auto PH) {
    constexpr int ph = decltype(PH)::value;
    CTS(3 + 4 * ph);
    __builtin_amdgcn_sched_barrier(0);
    sfor<GS[ph][4], GS[ph][5]>(gl);
    waitvmc<GON * (GS[ph][5] - GS[ph][4])>();
    SBAR();
    CTS(4 + 4 * ph);
    if (tid == 256) __hip_atomic_store(flags + ((ph + 1) * 32 + rt) * 64 + cb, 1u, __ATOMIC_RELAXED, __HIP_MEMORY_SCOPE_AGENT);
  };
  auto stat_acc = [&](const bf16x8& f, float& s, float& q) {
    u32x4 u = __builtin_bit_cast(u32x4, f);
#pragma unroll
    for (int d = 0; d < 4; d++) { s = dot2(u[d], 0x3F803F80u, s); q = dot2(u[d], u[d], q); }
  };
  auto stat_fin = [&](float s, float q) {
    s = xsum4(s); q = xsum4(q);
    float mean = s * (1.0f / D_);
    float var = fmaxf(q * (1.0f / D_) - mean * mean, 0.f);
    float ra = __builtin_amdgcn_rsqf(var + 1e-5f);
    float rb = ra * __builtin_amdgcn_rsqf(var * ra * ra + 1e-5f);
    if (g == 0) *(LDS_AS f32x4*)(lds + L_ST + row * 16) = f32x4{mean, ra, rb, 0.f};
    mean1 = mean; r1 = ra;
    return rb;
  };
  f32x4 ac[4];
  auto exch = [&]() {
#pragma unroll
    for (int p = 1; p < 4; p++) *(LDS_AS f32x4*)(lds + LX_A + (a ^ p) * 3072 + (p - 1) * 1024 + lane * 16) = ac[p];
    LBAR();
#pragma unroll
    for (int p = 1; p < 4; p++) ac[0] += *(const LDS_AS f32x4*)(lds + LX_A + a * 3072 + (p - 1) * 1024 + lane * 16);
  };
  auto tile4 = [&](auto PH, float& ss, float& sq) {
    constexpr int ph = decltype(PH)::value;
    constexpr int G0 = ph == 0 ? G2 : GS[ph - 1][5], PC = 4;
#pragma unroll
    for (int p = 0; p < 4; p++) ac[p] = f32x4{0, 0, 0, 0};
    sfor<0, 5>([&](auto CI) {
      constexpr int c = decltype(CI)::value;
      constexpr int GP = c == 0 ? G0 : GS[ph][c - 1];
      waitbarv<PC * (4 - c) + GON * (GP - G0)>();
      bf16x8 f[4], wf;
#pragma unroll
      for (int p = 0; p < 4; p++) f[p] = ldsfrag(aR[p] + c * 16384);
      wf = G[ph * 5 + c];
      bf16x8 e[3];
      if constexpr (ph == 1) {
#pragma unroll
        for (int d = 0; d < 3; d++) e[d] = ldsfrag(aSt[d] + c * 16384);
      }
      sfor<GP, GS[ph][c]>(gl);
#pragma unroll
      for (int p = 0; p < 4; p++) ac[p] = mfma(wf, f[p], ac[p]);
      if constexpr (ph == 1) {
        stat_acc(f[0], ss, sq);
#pragma unroll
        for (int d = 0; d < 3; d++) stat_acc(e[d], ss, sq);
      }
      __builtin_amdgcn_sched_barrier(0);
    });
  };
  float ss = 0.f, sq = 0.f;
  p2();
  __builtin_amdgcn_sched_barrier(0);
  sfor<5, G2>(gl);
  waitvmc<GON * (G2 - 5)>();
  SBAR();
  CTS(26);
  if (tid == 256) __hip_atomic_store(flags + rt * 64 + cb, 1u, __ATOMIC_RELAXED, __HIP_MEMORY_SCOPE_AGENT);
  poll(0);
  CTS(1);
  sfor<0, 20>([&](auto KI) { constexpr int ks = decltype(KI)::value; glds_sc1(ws + WS_O + ks * 4096, vT, ks * 4096 + a * 1024); });
  tile4(IC<0>{}, ss, sq);
  CTS(2);
  exch();
#pragma unroll
  for (int d = 0; d < 2; d++) hr[d] = cvtpk(bflo(hr[d]) + ac[0][2 * d], bfhi(hr[d]) + ac[0][2 * d + 1]);
  bst8(so640, WS_H, hr);
  publish(IC<0>{});
  poll(1);
  CTS(5);
  sfor<0, 20>([&](auto KI) { constexpr int ks = decltype(KI)::value; glds_sc1(ws + WS_H + ks * 4096, vT, ks * 4096 + a * 1024); });
  tile4(IC<1>{}, ss, sq);
  CTS(6);
  {
    float rs1 = stat_fin(ss, sq);
    exch();
    f32x4 c4 = *(const LDS_AS f32x4*)(lds + L_CS + (64 + 4 * g) * 4);
    float v[4];
#pragma unroll
    for (int e = 0; e < 4; e++) v[e] = gelu_f(rs1 * (ac[0][e] - mean1 * c4[e]));
    bst8(so640, WS_H1, u32x2{cvtpk(v[0], v[1]), cvtpk(v[2], v[3])});
  }
  publish(IC<1>{});
  {
    unsigned vSh[4];
#pragma unroll
    for (int k = 0; k < 4; k++) {
      const int sh = k == 0 ? 1 : k == 1 ? -1 : k == 2 ? 2 : -2;
      int r = row - sh + 64;
      vSh[k] = (unsigned)((rt + (r >> 6)) * T640 + ((r >> 4) & 3) * 1024 + (r & 15) * 16 + g * 256);
    }
    poll(2);
    CTS(9);
    sfor<0, 20>([&](auto KI) { constexpr int ks = decltype(KI)::value; glds_sc1(ws + WS_H1G + ks * 4096, vSh[ks / 5], ks * 4096 + a * 1024); });
  }
  tile4(IC<2>{}, ss, sq);
  CTS(10);
  exch();
#pragma unroll
  for (int d = 0; d < 2; d++)
    x1r[d] = cvtpk((bflo(hr[d]) - mean1) * r1 + ac[0][2 * d], (bfhi(hr[d]) - mean1) * r1 + ac[0][2 * d + 1]);
  bst8(so640, WS_X1, x1r);
  publish(IC<2>{});
  poll(3);
  CTS(13);
  sfor<0, 20>([&](auto KI) { constexpr int ks = decltype(KI)::value; glds_sc1(ws + WS_X1 + ks * 4096, vT, ks * 4096 + a * 1024); });
  {
    f32x4 ao[4], as[4];
#pragma unroll
    for (int u = 0; u < 4; u++) { ao[u] = f32x4{0, 0, 0, 0}; as[u] = f32x4{0, 0, 0, 0}; }
    const char *lE[4], *lO[4];
#pragma unroll
    for (int u = 0; u < 4; u++) {
      lE[u] = lds + b * 4096 + (u ^ a) * 1024 + lane * 16;
      lO[u] = lds + (1 - b) * 4096 + (u ^ a) * 1024 + lane * 16;
    }
    ss = 0.f; sq = 0.f;
    sfor<0, 5>([&](auto CI) {
      constexpr int c = decltype(CI)::value;
      constexpr int GP = c == 0 ? GS[2][5] : GS[3][c - 1];
      waitbarv<4 * (4 - c) + GON * (GP - GS[2][5])>();
      sfor<0, 4>([&](auto PI) {
        constexpr int p = decltype(PI)::value;
        bf16x8 f[4];
#pragma unroll
        for (int u = 0; u < 4; u++) f[u] = ldsfrag(((p & 1) ? lO[u] : lE[u]) + (4 * c + (p & ~1)) * 4096);
#pragma unroll
        for (int u = 0; u < 4; u++) ao[u] = mfma(G[15 + 6 * c + p], f[u], ao[u]);
        if constexpr ((p & 1) == 0) {
#pragma unroll
          for (int u = 0; u < 4; u++) as[u] = mfma(G[15 + 6 * c + 4 + (p >> 1)], f[u], as[u]);
        }
        stat_acc(f[0], ss, sq);
      });
      __builtin_amdgcn_sched_barrier(0);
      sfor<GP, GS[3][c]>(gl);
      __builtin_amdgcn_sched_barrier(0);
    });
    CTS(14);
    stat_fin(ss, sq);
    *(LDS_AS f32x4*)(lds + LX_A + (a ^ 1) * 3072 + lane * 16) = as[1];
    *(LDS_AS f32x4*)(lds + LX_A + (a ^ 1) * 3072 + 1024 + lane * 16) = as[3];
    LBAR();
    as[0] += *(const LDS_AS f32x4*)(lds + LX_A + a * 3072 + lane * 16);
    as[2] += *(const LDS_AS f32x4*)(lds + LX_A + a * 3072 + 1024 + lane * 16);
    int ln = lane; asm volatile("" : "+v"(ln));
    const int i = ln & 15, g = ln >> 4;
    const f32x4 co = *(const LDS_AS f32x4*)(lds + L_CS + 320 + (128 + 16 * a + 4 * g) * 4);
    const int st = 12 + (a >> 1);
    const bool shv = !(st == 13 && g >= 2);
    const f32x4 cs = *(const LDS_AS f32x4*)(lds + L_CS + 320 + (shv ? (16 * st + 4 * g) * 4 : 0));
    const int qo = cb * 27 + 16 + 2 * a + (g >> 1), qs = cb * 27 + 16 + 2 * (st - 8) + (g >> 1);
#pragma unroll
    for (int u = 0; u < 4; u++) {
      const int r = (u ^ a) * 16 + i;
      f32x4 s4 = *(const LDS_AS f32x4*)(lds + L_ST + r * 16);
      float v[4];
#pragma unroll
      for (int e = 0; e < 4; e++) v[e] = gelu_f(s4[1] * (ao[u][e] - s4[0] * co[e]));
      bst8((unsigned)(rt * T1728) + frag_off(qo, r) + (g & 1) * 8, WS_H2, u32x2{cvtpk(v[0], v[1]), cvtpk(v[2], v[3])});
      if ((u & 1) == 0) {
#pragma unroll
        for (int e = 0; e < 4; e++) v[e] = gelu_f(s4[1] * (as[u][e] - s4[0] * cs[e]));
        if (shv) bst8((unsigned)(rt * T1728) + frag_off(qs, r) + (g & 1) * 8, WS_H2, u32x2{cvtpk(v[0], v[1]), cvtpk(v[2], v[3])});
      }
    }
  }
  publish(IC<3>{});
  poll(4);
  CTS(17);
  {
    const unsigned vT7 = rt * T1728 + a * 1024 + lane * 16;
    auto issue = [&](auto CI) {
      constexpr int c = decltype(CI)::value;
      if constexpr (c < 14) {
        sfor<0, (c == 13 ? 2 : 4)>([&](auto KI) {
          constexpr int k = decltype(KI)::value;
          glds_sc1(ws + WS_H2 + (4 * c + k) * 4096, vT7, (c & 7) * 16384 + k * 4096 + a * 1024);
        });
      }
    };
    sfor<0, 6>(issue);
#pragma unroll
    for (int p = 0; p < 4; p++) ac[p] = f32x4{0, 0, 0, 0};
    sfor<0, 14>([&](auto CI) {
      constexpr int c = decltype(CI)::value;
      constexpr int last = c + 5 > 13 ? 13 : c + 5;
      constexpr int young = (last - c) * 4 - (last == 13 && c < 13 ? 2 : 0);
      waitbarv<young>();
      issue(IC<c + 6>{});
      if (c < 13 || a < 2) {
        bf16x8 f[4];
#pragma unroll
        for (int p = 0; p < 4; p++) f[p] = ldsfrag(aR[p] + (c & 7) * 16384);
#pragma unroll
        for (int p = 0; p < 4; p++) ac[p] = mfma(G[45 + c], f[p], ac[p]);
      }
      __builtin_amdgcn_sched_barrier(0);
    });
  }
  CTS(18);
  exch();
  {
    u32x2 o;
#pragma unroll
    for (int d = 0; d < 2; d++) o[d] = cvtpk(bflo(hr[d]) + bflo(x1r[d]) + ac[0][2 * d], bfhi(hr[d]) + bfhi(x1r[d]) + ac[0][2 * d + 1]);
    int ln = lane; asm volatile("" : "+v"(ln));
    *(u32x2*)(outp + (size_t)(rt * 64 + 16 * a + (ln & 15)) * D_ + cb * 80 + 64 + 4 * (ln >> 4)) = o;
  }
  CTS(19); CTS(20); CTS(21); CTS(22); CTS(23); CTS(27);
  if (w == 4) CTS_FLUSH(32);
}

extern "C" void kernel_launch(void* const* d_in, const int* in_sizes, int n_in, void* d_out, int out_size, void* d_ws,
                              size_t ws_size, hipStream_t stream) {
  const bf16_t* x = (const bf16_t*)d_in[0];
  const bf16_t* wqkv = (const bf16_t*)d_in[1];
  const bf16_t* wo = (const bf16_t*)d_in[2];
  const bf16_t* w1 = (const bf16_t*)d_in[4];
  const bf16_t* w2 = (const bf16_t*)d_in[5];
  const bf16_t* w3 = (const bf16_t*)d_in[6];
  const bf16_t* w4 = (const bf16_t*)d_in[7];
  char* ws = (char*)d_ws;
  bf16_t* out = (bf16_t*)d_out;
  (void)hipFuncSetAttribute((const void*)k_qkv, hipFuncAttributeMaxDynamicSharedMemorySize, K1_LDS);
  (void)hipFuncSetAttribute((const void*)k_chain, hipFuncAttributeMaxDynamicSharedMemorySize, CH_LDS);
  k_qkv<<<256, 512, K1_LDS, stream>>>(x, wqkv, wo, w1, w2, w3, w4, ws);
  k_chain<<<256, 512, CH_LDS, stream>>>(x, ws, out);
}
```

```cpp
#include <hip/hip_runtime.h>
#include <stdint.h>

typedef unsigned short bf16_t;
typedef __attribute__((ext_vector_type(8))) short bf16x8;
typedef __attribute__((ext_vector_type(4))) short s16x4;
typedef __attribute__((ext_vector_type(4))) float f32x4;
typedef __attribute__((ext_vector_type(2))) float f32x2;
typedef __attribute__((ext_vector_type(4))) unsigned u32x4;
typedef __attribute__((ext_vector_type(2))) unsigned u32x2;
#define LDS_AS __attribute__((address_space(3)))
#define GLB_AS __attribute__((address_space(1)))

constexpr int S_ = 2048, D_ = 640, F_ = 1728;

constexpr size_t MB_ = 1u << 20;
constexpr size_t WS_FL = 0, WS_REC = 65536, WS_CSP1 = 925696, WS_CSP3 = 976896,
                 WS_WOT = 1179648, WS_W1T = 1998848, WS_W2T = 2818048, WS_W3T = 3637248, WS_W4T = 5931008,
                 WS_PQ = 8142848, WS_O = 10764288, WS_H = 13385728,
                 WS_H1G = 16007168,
                 WS_H1 = WS_H1G + 81920, WS_X1 = 18792448, WS_H2 = 21413888;
constexpr int FL_BYTES = 49152;
constexpr int T640 = 81920, T1728 = 221184;
__device__ __forceinline__ unsigned frag_off(int q, int r) { return (unsigned)((q >> 2) * 4096 + (r >> 4) * 1024 + (q & 3) * 256 + (r & 15) * 16); }

#define STM_A 0
#define STM_X 0

template <int MODE> __device__ __forceinline__ void st16(void* p, u32x4 v) {
  if (MODE == 0) *(u32x4*)p = v;
  else if (MODE == 1) __builtin_nontemporal_store(v, (u32x4*)p);
  else if (MODE == 2) asm volatile("global_store_dwordx4 %0, %1, off sc0 sc1" ::"v"(p), "v"(v) : "memory");
  else asm volatile("global_store_dwordx4 %0, %1, off sc0 sc1 nt" ::"v"(p), "v"(v) : "memory");
}
template <int MODE> __device__ __forceinline__ void st8(void* p, u32x2 v) {
  if (MODE == 0) *(u32x2*)p = v;
  else if (MODE == 1) __builtin_nontemporal_store(v, (u32x2*)p);
  else if (MODE == 2) asm volatile("global_store_dwordx2 %0, %1, off sc0 sc1" ::"v"(p), "v"(v) : "memory");
  else asm volatile("global_store_dwordx2 %0, %1, off sc0 sc1 nt" ::"v"(p), "v"(v) : "memory");
}

__device__ __forceinline__ unsigned cvtpk(float lo, float hi) {
  unsigned r; asm("v_cvt_pk_bf16_f32 %0, %1, %2" : "=v"(r) : "v"(lo), "v"(hi)); return r;
}
__device__ __forceinline__ float bflo(unsigned v) { return __uint_as_float(v << 16); }
__device__ __forceinline__ float bfhi(unsigned v) { return __uint_as_float(v & 0xffff0000u); }
__device__ __forceinline__ void glds16(const void* g, char* l) {
  __builtin_amdgcn_global_load_lds((const GLB_AS void*)g, (LDS_AS void*)l, 16, 0, 0);
}
__device__ __forceinline__ f32x4 mfma(bf16x8 a, bf16x8 b, f32x4 c) {
  return __builtin_amdgcn_mfma_f32_16x16x32_bf16(a, b, c, 0, 0, 0);
}
__device__ __forceinline__ bf16x8 ldsfrag(const char* p) { return *(const LDS_AS bf16x8*)p; }
__device__ __forceinline__ s16x4 ldstr(const char* p) {
  typedef short v4i16_t __attribute__((ext_vector_type(4)));
  return __builtin_bit_cast(s16x4, __builtin_amdgcn_ds_read_tr16_b64_v4i16((LDS_AS v4i16_t*)p));
}
__device__ __forceinline__ float xsum4(float x) {
  auto s = __builtin_amdgcn_permlane16_swap(__float_as_uint(x), __float_as_uint(x), false, false);
  float y = __uint_as_float(s[0]) + __uint_as_float(s[1]);
  auto t = __builtin_amdgcn_permlane32_swap(__float_as_uint(y), __float_as_uint(y), false, false);
  return __uint_as_float(t[0]) + __uint_as_float(t[1]);
}
__device__ __forceinline__ float gelu_f(float x) {
  const float c0 = -2.302208198f;
  const float c1 = -0.1029432397f;
  float e = __builtin_amdgcn_exp2f(x * (c0 + c1 * x * x));
  return x * __builtin_amdgcn_rcpf(1.0f + e);
}
__device__ __forceinline__ float phi_f(float t) {
  float e = __builtin_amdgcn_exp2f(t * 1.4426950408889634f);
  return (t > 0.f ? t + 1.0f : e) + 1e-6f;
}
#define WAITBAR(N) asm volatile("s_waitcnt vmcnt(" #N ") lgkmcnt(0)\n\ts_barrier" ::: "memory")
#define LBAR() asm volatile("s_waitcnt lgkmcnt(0)\n\ts_barrier" ::: "memory")
template <int N> struct IC { static constexpr int value = N; };
#define TS_DECL
#define TS(k)
#define TS_FLUSH(kid, base)
#define CBAR() asm volatile("" ::: "memory")
template <int N> __device__ __forceinline__ void interleave_mfma_ds() {
#pragma unroll
  for (int k = 0; k < N; k++) { __builtin_amdgcn_sched_group_barrier(0x008, 1, 0); __builtin_amdgcn_sched_group_barrier(0x100, 1, 0); }
}

template <int I, int N, class F> __device__ __forceinline__ void sfor(F&& f) {
  if constexpr (I < N) { f(IC<I>{}); sfor<I + 1, N>(f); }
}
typedef __attribute__((ext_vector_type(2))) __bf16 bf16x2;
__device__ __forceinline__ float dot2(unsigned a, unsigned b, float c) {
  return __builtin_amdgcn_fdot2_f32_bf16(__builtin_bit_cast(bf16x2, a), __builtin_bit_cast(bf16x2, b), c, false);
}
__device__ __forceinline__ void glds_sc1(const char* sbase, unsigned voff, unsigned ldsoff) {
  asm volatile("s_mov_b32 m0, %2\n\tglobal_load_lds_dwordx4 %0, %1 sc1" ::"v"(voff), "s"(sbase), "s"(ldsoff) : "memory");
}
__device__ __forceinline__ void glds_pl(const char* sbase, unsigned voff, unsigned ldsoff) {
  asm volatile("s_mov_b32 m0, %2\n\tglobal_load_lds_dwordx4 %0, %1" ::"v"(voff), "s"(sbase), "s"(ldsoff) : "memory");
}
template <int N> __device__ __forceinline__ void waitvm() { asm volatile("s_waitcnt vmcnt(%0)" ::"n"(N) : "memory"); }
template <int N> __device__ __forceinline__ void waitbar() { asm volatile("s_waitcnt vmcnt(%0) lgkmcnt(0)\n\ts_barrier" ::"n"(N) : "memory"); }
#define SBAR() asm volatile("s_barrier" ::: "memory")

constexpr int PT_UNITS = 890;
constexpr int CSP3_KS = 8192, CSP3_CB = 256;
constexpr unsigned WS_DUM = 28u << 20;
struct PTS { const char* src; unsigned dst, csp; int ld, ks, cg, kind; };
__device__ __forceinline__ PTS pt_src(int u, const bf16_t* wo, const bf16_t* w1, const bf16_t* w2, const bf16_t* w3, const bf16_t* w4) {
  PTS s; int up, col0;
  s.ld = D_; s.kind = 0; s.csp = WS_DUM + u * 512; s.cg = 0;
  if (u < 570) {
    int nks = 20;
    if (u < 100) { s.src = (const char*)w1; s.dst = WS_W1T; up = u; }
    else if (u < 200) { s.src = (const char*)w2; s.dst = WS_W2T; up = u - 100; }
    else if (u < 300) { s.src = (const char*)wo; s.dst = WS_WOT; up = u - 200; }
    else { s.src = (const char*)w4; s.dst = WS_W4T; nks = 54; up = u - 300; }
    s.ks = up / 5; s.cg = up - s.ks * 5;
    col0 = s.cg * 128;
    if (u < 100) s.csp = WS_CSP1 + (s.ks * D_ + col0) * 4;
    s.dst += s.ks * 5120;
    s.cg |= nks << 8;
  } else {
    up = u - 570;
    s.ks = up >> 4; const int cb = (up >> 1) & 7; s.kind = 1 + (up & 1);
    s.src = (const char*)w3; s.ld = F_;
    col0 = cb * 216 + (up & 1) * 128;
    s.csp = WS_CSP3 + (s.ks * 8 + cb) * 1024 + (up & 1) * 512;
    s.dst = WS_W3T + (cb * 20 + s.ks) * 14336 + (up & 1) * 8192;
  }
  s.src += ((size_t)s.ks * 32 * s.ld + col0) * 2;
  return s;
}
#define STM_P 16
#define STM_Q 16

constexpr int K1_LDS = 5 * 32768;
#define K1_PF 1
#define K1_ORD 0
#define K1_PTNT 1
__device__ __forceinline__ void glds_nt(const char* sbase, unsigned voff, unsigned ldsoff) {
  asm volatile("s_mov_b32 m0, %2\n\tglobal_load_lds_dwordx4 %0, %1 nt" ::"v"(voff), "s"(sbase), "s"(ldsoff) : "memory");
}
#define K1_PT 1
__global__ __launch_bounds__(512, 1) void k_qkv(const bf16_t* __restrict__ x, const bf16_t* __restrict__ wqkv, const bf16_t* __restrict__ wo,
                                                const bf16_t* __restrict__ w1, const bf16_t* __restrict__ w2,
                                                const bf16_t* __restrict__ w3, const bf16_t* __restrict__ w4, char* __restrict__ ws) {
  extern __shared__ __attribute__((aligned(16))) char lds[];
  const int tid = threadIdx.x, lane = tid & 63;
  const int w = __builtin_amdgcn_readfirstlane(tid >> 6);
  const int xcd = blockIdx.x & 7, idx = blockIdx.x >> 3;
  const int rt = (xcd >> 1) * 4 + (idx >> 3), hp = (xcd & 1) * 8 + (idx & 7);
  const int i = lane & 15, g = lane >> 4, r = w & 3;
  const __amdgpu_buffer_rsrc_t rs = __builtin_amdgcn_make_buffer_rsrc(ws, 0, 0x10000000, 0x00020000);
  TS_DECL;
  TS(0);
  const unsigned voffA = (unsigned)((rt * 128 + 16 * w + (lane >> 3)) * 1280 + (((lane & 7) ^ (lane >> 3)) << 4));
  unsigned voffW;
  bool wld;
  {
    const int c = (lane & 15) ^ ((lane >> 4) << 1) ^ ((w & 1) << 3);
    const int col = c < 5 ? 8 * c : c <= 10 ? 640 + 8 * (c - 6) : c == 11 ? 1280 + 32 : 1280 + 8 * (c - 12);
    voffW = (unsigned)((8 * w + (lane >> 4)) * 3840 + (40 * hp + col) * 2);
    wld = c != 5;
  }
  auto G = [&](auto SI) {
    constexpr int st = decltype(SI)::value, sl = (st & 3) * 32768;
    glds_pl((const char*)x + st * 128, voffA, sl + w * 2048);
    glds_pl((const char*)x + st * 128 + 8 * 1280, voffA, sl + w * 2048 + 1024);
    if (wld) {
      glds_pl((const char*)wqkv + st * 64 * 3840, voffW, sl + 16384 + w * 2048);
      glds_pl((const char*)wqkv + st * 64 * 3840 + 4 * 3840, voffW, sl + 16384 + w * 2048 + 1024);
    }
  };
  unsigned pfd = 0;
  if (K1_PF) {
    const int l2 = w * 18 + lane - 46;
    const int st = lane < 46 ? w : l2 < 46 ? 8 : 9;
    const int rem = idx * 46 + (lane < 46 ? lane : l2 < 46 ? l2 : l2 < 92 ? l2 - 46 : 45);
    const int r2 = rem - 512, row = r2 / 15, l = r2 - row * 15;
    const char* p = rem < 512 ? (const char*)x + (size_t)((xcd >> 1) * 512 + rem) * 1280 + st * 128
                              : (const char*)wqkv + (size_t)(st * 64 + row) * 3840 + (l / 5) * 1280 + (xcd & 1) * 640 + (l % 5) * 128;
    asm volatile("global_load_dword %0, %1, off" : "=v"(pfd) : "v"(p) : "memory");
  }
  const bool hasu = K1_PT && w < 4 && blockIdx.x * 4 + r < PT_UNITS;
  PTS ps;
  auto ptload = [&]() {
    if (hasu) {
      ps = pt_src(blockIdx.x * 4 + r, wo, w1, w2, w3, w4);
      int c0 = i ^ (g << 1), c1 = c0 ^ 8;
      if (ps.kind == 2) { c0 = c0 < 10 ? c0 : 10; c1 = c1 < 10 ? c1 : 10; }
      const unsigned v0 = (unsigned)(g * ps.ld * 2 + c0 * 16), v1 = (unsigned)(g * ps.ld * 2 + c1 * 16);
#pragma unroll
      for (int j = 0; j < 8; j++) {
        if (K1_PTNT) glds_nt(ps.src + (size_t)j * 8 * ps.ld, ((j >> 1) & 1) ? v1 : v0, 131072 + r * 8192 + j * 1024);
        else glds_pl(ps.src + (size_t)j * 8 * ps.ld, ((j >> 1) & 1) ? v1 : v0, 131072 + r * 8192 + j * 1024);
      }
    }
  };
  sfor<0, 4>(G);
  ptload();
  if (!wld) {
    const u32x4 one = {0x3F803F80u, 0x3F803F80u, 0x3F803F80u, 0x3F803F80u};
#pragma unroll
    for (int k = 0; k < 8; k++) *(LDS_AS u32x4*)(lds + (k >> 1) * 32768 + 16384 + w * 2048 + (k & 1) * 1024 + lane * 16) = one;
  }
  int aoff[2];
#pragma unroll
  for (int m = 0; m < 2; m++) aoff[m] = ((2 * r + m) * 16 + i) * 128 + ((g ^ (i & 7)) << 4);
  float* tmean = (float*)(lds + 65536);
  float* trstd = tmean + 128;
  float* tcs = tmean + 256;
  char* red = lds + 65536 + 4096;
  const bf16x8 ones = {0x3F80, 0x3F80, 0x3F80, 0x3F80, 0x3F80, 0x3F80, 0x3F80, 0x3F80};
  const f32x4 z4 = {0, 0, 0, 0};

#define K1_GPOS 1
#define PT_LATE 1
#define K1_ILV 2
  auto ilv = [&](auto NT_) {
    constexpr int NT = decltype(NT_)::value;
    if constexpr (K1_ILV == 1) interleave_mfma_ds<2 * NT>();
    else {
#pragma unroll
      for (int k = 0; k < NT + 1; k++) { __builtin_amdgcn_sched_group_barrier(0x008, 1, 0); __builtin_amdgcn_sched_group_barrier(0x100, K1_ILV, 0); }
    }
  };
  auto role = [&](auto ISQ_, auto HASU_) {
    constexpr bool ISQ = decltype(ISQ_)::value, HASU = decltype(HASU_)::value;
    constexpr int NT = ISQ ? 3 : 5, NT0 = ISQ ? 0 : 3;
    int woff[NT];
#pragma unroll
    for (int u = 0; u < NT; u++) {
      const int nt = NT0 + u, q = i >> 2, p = i & 3, f = q | ((g & 1) << 2);
      woff[u] = 16384 + (8 * g + q) * 256 + (((nt * 2 + (p >> 1)) ^ (f << 1)) << 4) + (p & 1) * 8;
    }
    f32x4 acc[2][NT], cs[NT], gr[2] = {z4, z4};
#pragma unroll
    for (int u = 0; u < NT; u++) { acc[0][u] = z4; acc[1][u] = z4; cs[u] = z4; }
    struct Frag { bf16x8 a[2], w[NT]; };
    auto rd = [&](Frag& f, int slot, int kk) {
      const char* sl = lds + slot * 32768;
#pragma unroll
      for (int m = 0; m < 2; m++) f.a[m] = ldsfrag(sl + (aoff[m] ^ (kk * 64)));
#pragma unroll
      for (int u = 0; u < NT; u++) {
        s16x4 lo = ldstr(sl + woff[u] + kk * 8192), hi = ldstr(sl + woff[u] + kk * 8192 + 1024);
        f.w[u] = __builtin_shufflevector(lo, hi, 0, 1, 2, 3, 4, 5, 6, 7);
      }
    };
    auto mm = [&](const Frag& f) {
#pragma unroll
      for (int u = 0; u < NT; u++)
#pragma unroll
        for (int m = 0; m < 2; m++) acc[m][u] = ISQ ? mfma(f.w[u], f.a[m], acc[m][u]) : mfma(f.a[m], f.w[u], acc[m][u]);
      if constexpr (ISQ) { gr[0] = mfma(f.a[0], f.a[0], gr[0]); gr[1] = mfma(f.a[1], f.a[1], gr[1]); }
    };
    auto mcs = [&](const Frag& f, int sel) {
      if (r == sel) {
#pragma unroll
        for (int u = 0; u < NT; u++) cs[u] = ISQ ? mfma(f.w[u], ones, cs[u]) : mfma(ones, f.w[u], cs[u]);
      }
    };
    float cv[8];
    unsigned pa[8], po[8], pc[2];
    if constexpr (HASU) {
      const int q = i >> 2, p = i & 3, f = q | ((g & 1) << 2), pp = 2 * (p & 1) + (p >> 1);
      const unsigned sb = 131072 + r * 8192 + (8 * g + q) * 256;
      const int nb = (ps.cg >> 8) * 5120, cg8 = (ps.cg & 255) * 8;
      const unsigned vo32 = (unsigned)((g >> 1) * 1024 + (32 * (g & 1) + i) * 16), vo16 = (unsigned)(lane * 16);
#pragma unroll
      for (int j = 0; j < 8; j++) {
        const int Gi = cg8 + j, cb = Gi / 5, r5 = Gi - cb * 5;
        const bool n32 = ps.kind == 0 ? r5 < 4 : ps.kind == 1;
        const int jj = ps.kind == 2 && j > 5 ? 5 : j;
        const unsigned dst = ps.kind == 0 ? cb * nb + (r5 < 4 ? (r5 >> 1) * 2048 + (r5 & 1) * 256 : 4096) : ps.kind == 1 ? (j >> 1) * 2048 + (j & 1) * 256 : jj * 1024;
        const int pe = n32 ? pp : p;
        pa[j] = sb + (((jj * 2 + (pe >> 1)) ^ (f << 1)) << 4) + (pe & 1) * 8;
        po[j] = ps.dst + dst + (n32 ? vo32 : vo16);
        asm volatile("" : "+v"(pa[j]), "+v"(po[j]));
      }
#pragma unroll
      for (int h = 0; h < 2; h++) {
        const int Gj = 4 * h + g, Gi = cg8 + Gj;
        const bool n32 = ps.kind == 0 ? Gi - (Gi / 5) * 5 < 4 : ps.kind == 1;
        const int ci = n32 ? 8 * ((i >> 2) & 1) + 4 * (i >> 3) + (i & 3) : i;
        const int col = Gj * 16 + ci;
        const bool ok = !(ps.kind == 2 && col >= 88);
        pc[h] = ok ? ps.csp + (unsigned)col * 4 : WS_DUM + (blockIdx.x * 4 + r) * 512 + lane * 4;
        asm volatile("" : "+v"(pc[h]));
      }
    }
    auto pt_rd = [&](auto J, bf16x8& v) {
      constexpr int j = decltype(J)::value;
      s16x4 lo = ldstr(lds + pa[j]), hi = ldstr(lds + pa[j] + 1024);
      v = __builtin_shufflevector(lo, hi, 0, 1, 2, 3, 4, 5, 6, 7);
    };
    auto pt_st = [&](auto J, const bf16x8& v) {
      constexpr int j = decltype(J)::value;
      __builtin_amdgcn_raw_buffer_store_b128(__builtin_bit_cast(u32x4, v), rs, po[j], 0, STM_P);
      cv[j] = mfma(ones, v, z4)[0];
    };
    auto pt_cs = [&](auto H) {
      constexpr int h = decltype(H)::value;
      const float s = g == 0 ? cv[4 * h] : g == 1 ? cv[4 * h + 1] : g == 2 ? cv[4 * h + 2] : cv[4 * h + 3];
      __builtin_amdgcn_raw_buffer_store_b32(__float_as_uint(s), rs, pc[h], 0, STM_P);
    };
    bf16x8 pv0, pv1, pv2;
    Frag f0, f1;
    waitbar<12 + (HASU ? 8 : 0)>();
    asm volatile("" ::"v"(pfd));
    TS(1);
    rd(f0, 0, 0);
    sfor<0, 10>([&](auto SI) {
      constexpr int s = decltype(SI)::value;
      rd(f1, s & 3, 1);
      mm(f0);
      ilv(IC<NT>{});
      __builtin_amdgcn_sched_barrier(0);
      mcs(f0, (2 * s) & 3);
      __builtin_amdgcn_sched_barrier(0);
      if constexpr (s < 9) {
        constexpr int b = s + 1;
        constexpr int n4 = HASU ? 2 : 0, n5 = HASU ? 3 : 0;
        constexpr int yo = b <= 3 ? 8 + (HASU ? 8 : 0) : PT_LATE ? (b <= 7 ? 8 : b == 8 ? 4 + n5 : HASU ? 7 : 0)
                         : b == 4 ? 8 : b == 5 ? 8 + n4 : b == 6 ? 8 + n4 + n5 : b == 7 ? 8 + 2 * n4 + n5 : b == 8 ? 4 + 2 * n5 + n4 : n4 + n5;
        waitbar<yo>();
        if constexpr (b + 3 < 10 && (K1_GPOS == 0 || (K1_GPOS == 1 && ISQ))) G(IC<b + 3>{});
        rd(f0, b & 3, 0);
        if constexpr (HASU && !PT_LATE && b >= 4 && b <= 7) { pt_rd(IC<2 * (b - 4)>{}, pv0); pt_rd(IC<2 * (b - 4) + 1>{}, pv1); }
        if constexpr (HASU && PT_LATE && b >= 7) { pt_rd(IC<3 * (b - 7)>{}, pv0); pt_rd(IC<3 * (b - 7) + 1>{}, pv1); if constexpr (b < 9) pt_rd(IC<3 * (b - 7) + 2>{}, pv2); }
      }
      mm(f1);
      if constexpr (s < 9) ilv(IC<NT>{});
      __builtin_amdgcn_sched_barrier(0);
      mcs(f1, (2 * s + 1) & 3);
      __builtin_amdgcn_sched_barrier(0);
      if constexpr (HASU && PT_LATE && s >= 6 && s <= 8) {
        pt_st(IC<3 * (s - 6)>{}, pv0); pt_st(IC<3 * (s - 6) + 1>{}, pv1);
        if constexpr (s < 8) pt_st(IC<3 * (s - 6) + 2>{}, pv2);
        if constexpr (s >= 7) pt_cs(IC<s - 7>{});
        __builtin_amdgcn_sched_barrier(0);
      }
      if constexpr (HASU && !PT_LATE && s >= 3 && s <= 6) {
        pt_st(IC<2 * (s - 3)>{}, pv0); pt_st(IC<2 * (s - 3) + 1>{}, pv1);
        if constexpr ((s & 1) == 0) pt_cs(IC<(s - 3) / 2>{});
        __builtin_amdgcn_sched_barrier(0);
      }
      if constexpr (s + 4 < 10 && ((K1_GPOS == 1 && !ISQ) || K1_GPOS == 2)) G(IC<s + 4>{});
      __builtin_amdgcn_sched_barrier(0);
    });
    TS(2);
    if constexpr (ISQ) {
#pragma unroll
      for (int m = 0; m < 2; m++) {
        auto sw = __builtin_amdgcn_permlane32_swap(__float_as_uint(acc[m][2][0]), __float_as_uint(acc[m][2][0]), false, false);
        const float mean = __uint_as_float(sw[1]) * (1.0f / D_);
        const float q = (i & 3) == 0 ? gr[m][0] : (i & 3) == 1 ? gr[m][1] : (i & 3) == 2 ? gr[m][2] : gr[m][3];
        const float rstd = __builtin_amdgcn_rsqf(fmaxf(q * (1.0f / D_) - mean * mean, 0.f) + 1e-5f);
        if ((i >> 2) == g) { tmean[32 * r + 16 * m + i] = mean; trstd[32 * r + 16 * m + i] = rstd; }
      }
      if (i == 0) {
#pragma unroll
        for (int u = 0; u < 3; u++) *(f32x4*)(tcs + r * 48 + u * 16 + 4 * g) = cs[u];
      }
      LBAR();
      TS(3);
      f32x4 c4[3];
      float mean[2], rstd[2];
#pragma unroll
      for (int m = 0; m < 2; m++) { mean[m] = tmean[32 * r + 16 * m + i]; rstd[m] = trstd[32 * r + 16 * m + i]; }
#pragma unroll
      for (int u = 0; u < 3; u++) {
        const float* p = tcs + u * 16 + 4 * g;
        c4[u] = (*(const f32x4*)p + *(const f32x4*)(p + 48)) + (*(const f32x4*)(p + 96) + *(const f32x4*)(p + 144));
      }
      unsigned pk[2][3][2];
#pragma unroll
      for (int m = 0; m < 2; m++)
#pragma unroll
        for (int u = 0; u < 3; u++) {
          float v[4];
#pragma unroll
          for (int e = 0; e < 4; e++) v[e] = phi_f(rstd[m] * (acc[m][u][e] - mean[m] * c4[u][e]));
          pk[m][u][0] = cvtpk(v[0], v[1]); pk[m][u][1] = cvtpk(v[2], v[3]);
        }
      const unsigned qb = (unsigned)(((rt * 2 + (r >> 1)) * 8 + (hp >> 1)) * 10240 + (hp & 1) * 5120 + ((r & 1) * 32 + i) * 16);
#pragma unroll
      for (int m = 0; m < 2; m++) {
        auto x0 = __builtin_amdgcn_permlane16_swap(pk[m][0][0], pk[m][1][0], false, false);
        auto x1 = __builtin_amdgcn_permlane16_swap(pk[m][0][1], pk[m][1][1], false, false);
        __builtin_amdgcn_raw_buffer_store_b128(u32x4{x0[0], x1[0], x0[1], x1[1]}, rs, qb + m * 256 + (2 * (g & 1) + (g >> 1)) * 1024, WS_PQ, STM_Q);
      }
      {
        auto x0 = __builtin_amdgcn_permlane16_swap(pk[0][2][0], pk[1][2][0], false, false);
        auto x1 = __builtin_amdgcn_permlane16_swap(pk[0][2][1], pk[1][2][1], false, false);
        if (g < 2) __builtin_amdgcn_raw_buffer_store_b128(u32x4{x0[0], x1[0], x0[1], x1[1]}, rs, qb + (g & 1) * 256 + 4 * 1024, WS_PQ, STM_Q);
      }
      {
        const int t = (blockIdx.x * 4 + r) * 64 + lane;
        if (t < FL_BYTES / 16) __builtin_amdgcn_raw_buffer_store_b128(u32x4{0, 0, 0, 0}, rs, t * 16, WS_FL, STM_Q);
        else if (t < FL_BYTES / 16 + 320) {
          const int z = t - FL_BYTES / 16, q = z >> 2, k = z & 3;
          const unsigned o = k < 2 ? frag_off(q, 62 + k) : 33u * T640 + frag_off(q, k - 2);
          __builtin_amdgcn_raw_buffer_store_b128(u32x4{0, 0, 0, 0}, rs, o, WS_H1G, STM_Q);
        }
      }
      LBAR();
    } else {
      if (g == 0) {
#pragma unroll
        for (int u = 0; u < 5; u++) tcs[192 + r * 80 + u * 16 + i] = cs[u][0];
      }
      LBAR();
      TS(3);
      bf16x8 op[5];
      f32x4 m4[2], r4[2];
#pragma unroll
      for (int m = 0; m < 2; m++) { m4[m] = *(const f32x4*)(tmean + 32 * r + 16 * m + 4 * g); r4[m] = *(const f32x4*)(trstd + 32 * r + 16 * m + 4 * g); }
#pragma unroll
      for (int u = 0; u < 5; u++) {
        const float* p = tcs + 192 + u * 16 + i;
        const float c = (p[0] + p[80]) + (p[160] + p[240]);
        u32x4 o;
#pragma unroll
        for (int m = 0; m < 2; m++) {
          float v[4];
#pragma unroll
          for (int e = 0; e < 4; e++) {
            float a = r4[m][e] * (acc[m][u][e] - m4[m][e] * c);
            v[e] = u < 2 ? phi_f(a) : u == 2 ? (i < 8 ? phi_f(a) : a) : a;
          }
          o[2 * m] = cvtpk(v[0], v[1]); o[2 * m + 1] = cvtpk(v[2], v[3]);
        }
        op[u] = __builtin_bit_cast(bf16x8, o);
      }
      char* rw = red + r * 10240 + lane * 16;
      *(f32x4*)(rw + 0 * 1024) = mfma(op[0], op[3], z4);
      *(f32x4*)(rw + 1 * 1024) = mfma(op[0], op[4], z4);
      *(f32x4*)(rw + 2 * 1024) = mfma(op[1], op[3], z4);
      *(f32x4*)(rw + 3 * 1024) = mfma(op[1], op[4], z4);
      *(f32x4*)(rw + 4 * 1024) = mfma(op[1], op[2], z4);
      *(f32x4*)(rw + 5 * 1024) = mfma(op[2], op[4], z4);
      *(f32x4*)(rw + 6 * 1024) = mfma(op[2], op[2], z4);
      *(f32x4*)(rw + 7 * 1024) = mfma(op[0], ones, z4);
      *(f32x4*)(rw + 8 * 1024) = mfma(op[1], ones, z4);
      *(f32x4*)(rw + 9 * 1024) = mfma(op[2], ones, z4);
      LBAR();
      const int t = tid - 256;
      if (t < 210) {
        const int h = t >= 105, rem = t - 105 * h;
        const bool isz = rem >= 100;
        const int e = isz ? 0 : rem / 5, dq = isz ? rem - 100 : rem - e * 5;
        int dt, gg, et, ii;
        if (h == 0) { dt = dq < 4 ? 0 : 1; gg = dq < 4 ? dq : 0; et = e < 16 ? 0 : 1; ii = e & 15; }
        else { dt = dq < 3 ? 1 : 2; gg = dq < 3 ? dq + 1 : dq - 3; et = e < 12 ? 1 : 2; ii = e < 12 ? e + 4 : e - 4; }
        const int tile = isz ? 7 + dt : dt == 0 ? et : dt == 1 ? 2 + et : 4 + et;
        const char* p = red + tile * 1024 + (gg * 16 + (isz ? 0 : ii)) * 16;
        f32x4 s = *(const f32x4*)p;
#pragma unroll
        for (int k = 1; k < 4; k++) s += *(const f32x4*)(p + k * 10240);
        __builtin_amdgcn_raw_buffer_store_b128(__builtin_bit_cast(u32x4, s), rs, (unsigned)(((rt * 16 + hp) * 210 + t) * 16), WS_REC, STM_Q);
      }
    }
    TS(5);
  };
  if (w >= 4) role(IC<0>{}, IC<0>{});
  else if (hasu) role(IC<1>{}, IC<K1_PT>{});
  else role(IC<1>{}, IC<0>{});
  if (w == 0) TS_FLUSH(1, 0);
  if (w == 4) TS_FLUSH(1, 8);
}

typedef __attribute__((ext_vector_type(16))) float f32x16;
typedef GLB_AS unsigned gu32;
constexpr int LX_M = 131072, LX_A = 147456, L_ST = 159744, L_CS = 160768, L_T = 131072, CH_LDS = 162048;
#define CH_RD 28
#define CH_QZ 4
constexpr int RD = CH_RD, NFR = 154, QZ = CH_QZ;
constexpr int PBND[6] = {0, 20, 40, 60, 100, 154};
#define CH_NPRE 4
#define CH_SL 1
#define CH_DEN 2
#define CH_T2 88
constexpr int NPRE = CH_NPRE;
struct LSched { int step[NFR]; int pre[5]; int pub[5]; int pre2, pub2; };
constexpr LSched make_sched() {
  LSched s{};
  s.pre2 = 20 + NPRE; s.pub2 = RD < 40 ? RD : 40;
  int issued = s.pub2;
  constexpr int TG[5] = {40, 60, CH_T2, 128, 154};
  for (int p = 0; p < 5; p++) {
    for (int f = PBND[p]; f < PBND[p + 1]; f++) {
      const int den = p < 3 ? CH_DEN : 1;
      bool go = (f - PBND[p]) % den == 0 && issued < TG[p] && issued < f + 1 + RD;
      if (issued >= PBND[p + 1] && f >= PBND[p + 1] - QZ) go = false;
      if (issued < PBND[p + 1] && issued <= f + 4) go = true;
      if (go) issued++;
      s.step[f] = issued;
    }
    int lim = PBND[p + 1] + RD; if (lim > TG[p]) lim = TG[p];
    if (lim < issued) lim = issued;
    s.pre[p] = issued + NPRE < lim ? issued + NPRE : lim;
    issued = lim;
    s.pub[p] = issued;
  }
  return s;
}
constexpr LSched LS = make_sched();
constexpr int MON = 1;
constexpr int GON = 1;

__device__ __forceinline__ f32x16 mfma32(bf16x8 a, bf16x8 b, f32x16 c) {
  return __builtin_amdgcn_mfma_f32_32x32x16_bf16(a, b, c, 0, 0, 0);
}
template <int N> __device__ __forceinline__ void waitvmc() { __builtin_amdgcn_s_waitcnt((N & 15) | ((N >> 4) << 14) | 0x0F70); }
__device__ __forceinline__ void glds_pf(const char* sbase, unsigned voff, unsigned ldsoff) {
  asm volatile("s_mov_b32 m0, %2\n\tglobal_load_lds_dword %0, %1" ::"v"(voff), "s"(sbase), "s"(ldsoff) : "memory");
}
template <int N> __device__ __forceinline__ void waitbarv() { asm volatile("s_waitcnt vmcnt(%0)\n\ts_barrier" ::"n"(N) : "memory"); }
#define VM0() __builtin_amdgcn_s_waitcnt(0x0F70)
#define CTS_DECL
#define CTS(k)
#define CTS_FLUSH(base)

__global__ __launch_bounds__(512, 1) void k_chain(const bf16_t* __restrict__ x, char* __restrict__ ws, bf16_t* __restrict__ outp) {
  extern __shared__ __attribute__((aligned(16))) char lds[];
  const int tid = threadIdx.x, lane = tid & 63;
  const int w = __builtin_amdgcn_readfirstlane(tid >> 6);
  const int xcd = blockIdx.x & 7, idx = blockIdx.x >> 3;
  const int rt = xcd * 4 + (idx >> 3), cb = idx & 7;
#define P2MAP 1
  const int rt2 = P2MAP ? idx : rt, cb2 = P2MAP ? xcd : cb;
  const __amdgpu_buffer_rsrc_t rs = __builtin_amdgcn_make_buffer_rsrc(ws, 0, 0x10000000, 0x00020000);
  auto bload = [&](unsigned v, unsigned so) { return __builtin_bit_cast(bf16x8, __builtin_amdgcn_raw_buffer_load_b128(rs, v, so, 0)); };
  auto bst16 = [&](unsigned v, unsigned so, u32x4 d) { __builtin_amdgcn_raw_buffer_store_b128(d, rs, v, so, 16); };
  auto bst8 = [&](unsigned v, unsigned so, u32x2 d) { __builtin_amdgcn_raw_buffer_store_b64(d, rs, v, so, 16); };
  CTS_DECL;
  CTS(0);

  f32x4 rv[20];
  bf16x8 bq[3];
  int rn = 16;
  {
    const int hpl = tid >= 210;
    unsigned rb = (unsigned)WS_REC + (unsigned)(((2 * cb2 + hpl) * 210 + tid - 210 * hpl) * 16), rst = 53760;
    if (tid >= 420) {
      rn = tid < 440 || (tid >= 448 && tid < 502) ? 20 : 0;
      rst = rn == 0 ? 0 : tid < 448 ? 2560 : CSP3_KS;
      rb = rn == 0 ? (unsigned)WS_REC : tid < 448 ? (unsigned)WS_CSP1 + (cb * 80 + 4 * (tid - 420)) * 4 : (unsigned)WS_CSP3 + (cb * CSP3_CB + 4 * (tid - 448)) * 4;
    }
#pragma unroll
    for (int p = 0; p < 16; p++) rv[p] = __builtin_bit_cast(f32x4, __builtin_amdgcn_raw_buffer_load_b128(rs, rb + p * rst, 0, 0));
    if (w >= 6) {
#pragma unroll
      for (int p = 16; p < 20; p++) rv[p] = __builtin_bit_cast(f32x4, __builtin_amdgcn_raw_buffer_load_b128(rs, rb + (rn == 20 ? p * rst : 0), 0, 0));
    }
    const int g = lane >> 4;
#pragma unroll
    for (int s = 0; s < 3; s++) {
      const int c = 4 * s + g < 9 ? 4 * s + g : 9;
      bq[s] = bload((unsigned)((rt2 * 8 + cb2) * 10240 + c * 1024 + (16 * (w & 3) + (lane & 15)) * 16), WS_PQ);
    }
    CBAR();
#pragma unroll
    for (int k = 0; k < 3; k++)
      if (tid + 512 * k < 1092) *(LDS_AS u32x4*)(lds + L_T + (tid + 512 * k) * 16) = u32x4{0, 0, 0, 0};
  }
  auto p2 = [&]() {
    const int i = lane & 15, g = lane >> 4;
    LBAR();
    f32x4 s = ((rv[0] + rv[1]) + (rv[2] + rv[3])) + ((rv[4] + rv[5]) + (rv[6] + rv[7])) +
              (((rv[8] + rv[9]) + (rv[10] + rv[11])) + ((rv[12] + rv[13]) + (rv[14] + rv[15])));
    if (w >= 6) { if (rn == 20) s += (rv[16] + rv[17]) + (rv[18] + rv[19]); }
    CTS(24);
    if (tid < 420) {
      const int hpl = tid >= 210, t = tid - 210 * hpl, h = t >= 105, rem = t - 105 * h, hl = 2 * hpl + h;
      const int e = rem / 5, dq = rem - e * 5;
      const int n = e == 20 ? 80 + hl : hl * 20 + e;
      *(LDS_AS u32x2*)(lds + L_T + n * 208 + (hl * 20 + 4 * dq) * 2) = u32x2{cvtpk(s[0], s[1]), cvtpk(s[2], s[3])};
    } else if (rn == 20) {
      *(LDS_AS f32x4*)(lds + L_CS + (tid < 448 ? (tid - 420) * 16 : 320 + (tid - 448) * 16)) = s;
    }
    LBAR();
    const char* tp = lds + L_T + i * 208 + g * 16;
    const char* zp = lds + L_T + (80 + (i & 3)) * 208 + g * 16;
    const f32x4 z4 = {0, 0, 0, 0};
    f32x4 dn = mfma(ldsfrag(zp), bq[0], z4);
    dn = mfma(ldsfrag(zp + 64), bq[1], dn);
    dn = mfma(ldsfrag(zp + 128), bq[2], dn);
        auto fin = [&](auto UI, const f32x4& nu) {
      constexpr int u = decltype(UI)::value;
      const float d = u == 0 ? dn[0] : u == 1 ? (g == 0 ? dn[0] : dn[1]) : u == 2 ? (g < 2 ? dn[1] : dn[2]) : u == 3 ? (g < 3 ? dn[2] : dn[3]) : dn[3];
      const float inv = __builtin_amdgcn_rcpf(d + 1e-6f);
      const unsigned o = (unsigned)(rt2 * T640) + frag_off(cb2 * 10 + 2 * u + (g >> 1), 16 * (w & 3) + i) + (g & 1) * 8;
      bst8(o, WS_O, u32x2{cvtpk(nu[0] * inv, nu[1] * inv), cvtpk(nu[2] * inv, nu[3] * inv)});
    };
    if (w < 4) {
      f32x4 n0 = mfma(ldsfrag(tp), bq[0], z4);
      f32x4 n1 = mfma(ldsfrag(tp + 16 * 208), bq[0], z4);
      f32x4 n2 = mfma(ldsfrag(tp + 32 * 208), bq[0], z4);
      n1 = mfma(ldsfrag(tp + 16 * 208 + 64), bq[1], n1);
      n2 = mfma(ldsfrag(tp + 32 * 208 + 64), bq[1], n2);
      fin(IC<0>{}, n0); fin(IC<1>{}, n1); fin(IC<2>{}, n2);
    } else {
      f32x4 n3 = mfma(ldsfrag(tp + 48 * 208 + 64), bq[1], z4);
      f32x4 n4 = mfma(ldsfrag(tp + 64 * 208 + 64), bq[1], z4);
      n3 = mfma(ldsfrag(tp + 48 * 208 + 128), bq[2], n3);
      n4 = mfma(ldsfrag(tp + 64 * 208 + 128), bq[2], n4);
      fin(IC<3>{}, n3); fin(IC<4>{}, n4);
    }
    CTS(25);
  };

  if (w < 4) {
    const int t = w & 1, kh = w >> 1, hh = lane >> 5, l31 = lane & 31;
    const unsigned vA = cb * 102400 + kh * 5120 + t * 2048 + lane * 16;
    const unsigned vB = cb * 286720 + w * 2048 + lane * 16;
    const unsigned vC = cb * 276480 + kh * 5120 + t * 2048 + lane * 16;
    bf16x8 ring[RD];
    auto wl = [&](auto FI) {
      constexpr int F = decltype(FI)::value;
      constexpr int ph = F < 20 ? 0 : F < 40 ? 1 : F < 60 ? 2 : F < 100 ? 3 : 4;
      constexpr int f = F - PBND[ph];
      constexpr unsigned so = ph == 0 ? WS_WOT + (f >> 1) * 10240 : ph == 1 ? WS_W1T + (f >> 1) * 10240 : ph == 2 ? WS_W2T + (f >> 1) * 10240
                            : ph == 3 ? WS_W3T + (f >> 1) * 14336 : WS_W4T + (f >> 1) * 10240;
      ring[F % RD] = bload((ph < 3 ? vA : ph == 3 ? vB : vC) + (f & 1) * 1024, so);
    };
    sfor<0, 20>(wl);
    const int row = 32 * kh + l31;
    const int colq = cb * 10 + 4 * t + hh;
    u32x4 hr[2], x1r[2];
    {
      const bf16_t* xp = x + (size_t)(rt * 64 + row) * D_ + colq * 8;
      hr[0] = *(const u32x4*)xp; hr[1] = *(const u32x4*)(xp + 16);
    }
    const unsigned so640[2] = {(unsigned)(rt * T640) + frag_off(colq, row), (unsigned)(rt * T640) + frag_off(colq + 2, row)};
    const unsigned lp = ((lane >> 4) & 1) * 1024 + hh * 256 + (lane & 15) * 16;
    const char* aA[2] = {lds + kh * 4096 + kh * 2048 + lp, lds + kh * 4096 + (kh ^ 1) * 2048 + lp};
    const char* aL = lds + lp;
    f32x16 acc[2];
    bf16x8 af[3][2];
    float mean1 = 0.f, r1 = 0.f;

    auto gemm = [&](auto PH) {
      constexpr int ph = decltype(PH)::value;
      constexpr int F0 = PBND[ph], NS = PBND[ph + 1] - F0;
      auto rdA = [&](auto SI) {
        constexpr int s = decltype(SI)::value;
        if constexpr (ph == 3) {
          constexpr int o = (s >> 1) * 4096 + (s & 1) * 512;
          af[s % 3][0] = ldsfrag(aL + o); af[s % 3][1] = ldsfrag(aL + o + 2048);
        } else {
          constexpr int o = (ph == 4 ? ((s >> 2) & 7) * 16384 + ((s >> 1) & 1) * 8192 : (s >> 1) * 8192) + (s & 1) * 512;
          af[s % 3][0] = ldsfrag(aA[0] + o); af[s % 3][1] = ldsfrag(aA[1] + o);
        }
      };
#pragma unroll
      for (int e = 0; e < 16; e++) { acc[0][e] = 0.f; acc[1][e] = 0.f; }
      SBAR();
      CTS(1 + 4 * ph);
      rdA(IC<0>{}); rdA(IC<1>{});
      __builtin_amdgcn_sched_barrier(0);
      sfor<0, NS>([&](auto SI) {
        constexpr int s = decltype(SI)::value;
        if constexpr (s + 2 < NS) {
          if constexpr (((s + 2) & (ph == 3 ? 7 : 3)) == 0) SBAR();
          rdA(IC<s + 2>{});
        }
        acc[0] = mfma32(ring[(F0 + s) % RD], af[s % 3][0], acc[0]);
        acc[1] = mfma32(ring[(F0 + s) % RD], af[s % 3][1], acc[1]);
        constexpr int l0 = (F0 + s) == 0 ? LS.pub2 : (s == 0 ? LS.pub[ph > 0 ? ph - 1 : 0] : LS.step[F0 + s - (F0 + s > 0 ? 1 : 0)]);
        sfor<l0, LS.step[F0 + s]>(wl);
        __builtin_amdgcn_sched_barrier(0);
      });
      CTS(2 + 4 * ph);
    };
    auto exch = [&]() {
      char* xw = lds + LX_M + w * 4096 + lane * 16;
      const char* xr = lds + LX_M + (w ^ 2) * 4096 + lane * 16;
#pragma unroll
      for (int j = 0; j < 4; j++) *(LDS_AS f32x4*)(xw + j * 1024) = f32x4{acc[1][4 * j], acc[1][4 * j + 1], acc[1][4 * j + 2], acc[1][4 * j + 3]};
      LBAR();
#pragma unroll
      for (int j = 0; j < 4; j++) {
        f32x4 v = *(const LDS_AS f32x4*)(xr + j * 1024);
#pragma unroll
        for (int e = 0; e < 4; e++) acc[0][4 * j + e] += v[e];
      }
    };
    auto wlp = [&](auto FI) { wl(FI); __builtin_amdgcn_s_sleep(CH_SL); __builtin_amdgcn_sched_barrier(0); };
    auto publish = [&](auto PH) {
      constexpr int ph = decltype(PH)::value;
      CTS(3 + 4 * ph);
      __builtin_amdgcn_sched_barrier(0);
      sfor<LS.step[PBND[ph + 1] - 1], LS.pre[ph]>(wlp);
      waitvmc<MON * (LS.pre[ph] - LS.step[PBND[ph + 1] - 1])>();
      SBAR();
      CTS(4 + 4 * ph);
      sfor<LS.pre[ph], LS.pub[ph]>(wlp);
      __builtin_amdgcn_sched_barrier(0);
    };
    auto res = [&](const u32x4& r, int e) { return (e & 1) ? bfhi(r[e >> 1]) : bflo(r[e >> 1]); };

    p2();
    __builtin_amdgcn_sched_barrier(0);
    sfor<20, LS.pre2>(wlp);
    waitvmc<MON * (LS.pre2 - 20)>();
    SBAR();
    CTS(26);
    sfor<LS.pre2, LS.pub2>(wlp);
    __builtin_amdgcn_sched_barrier(0);
    gemm(IC<0>{});
    exch();
#pragma unroll
    for (int c = 0; c < 2; c++) {
#pragma unroll
      for (int d = 0; d < 4; d++)
        hr[c][d] = cvtpk(res(hr[c], 2 * d) + acc[0][8 * c + 2 * d], res(hr[c], 2 * d + 1) + acc[0][8 * c + 2 * d + 1]);
      bst16(so640[c], WS_H, hr[c]);
    }
    publish(IC<0>{});
    gemm(IC<1>{});
    exch();
    {
      f32x4 st = *(const LDS_AS f32x4*)(lds + L_ST + row * 16);
      mean1 = st[0]; r1 = st[1];
      const float rs1 = st[2];
#pragma unroll
      for (int c = 0; c < 2; c++) {
        const char* cp = lds + L_CS + (32 * t + 16 * c + 8 * hh) * 4;
        f32x4 c0 = *(const LDS_AS f32x4*)cp, c1 = *(const LDS_AS f32x4*)(cp + 16);
        float v[8];
#pragma unroll
        for (int e = 0; e < 8; e++) v[e] = gelu_f(rs1 * (acc[0][8 * c + e] - mean1 * (e < 4 ? c0[e & 3] : c1[e & 3])));
        bst16(so640[c], WS_H1, u32x4{cvtpk(v[0], v[1]), cvtpk(v[2], v[3]), cvtpk(v[4], v[5]), cvtpk(v[6], v[7])});
      }
    }
    publish(IC<1>{});
    gemm(IC<2>{});
    exch();
#pragma unroll
    for (int c = 0; c < 2; c++) {
#pragma unroll
      for (int d = 0; d < 4; d++)
        x1r[c][d] = cvtpk((res(hr[c], 2 * d) - mean1) * r1 + acc[0][8 * c + 2 * d], (res(hr[c], 2 * d + 1) - mean1) * r1 + acc[0][8 * c + 2 * d + 1]);
      bst16(so640[c], WS_X1, x1r[c]);
    }
    publish(IC<2>{});
    gemm(IC<3>{});
    LBAR();
    {
      const int q0 = cb * 27 + 4 * w + hh;
#pragma unroll
      for (int c = 0; c < 2; c++) {
        const char* cp = lds + L_CS + 320 + (32 * w + 16 * c + 8 * hh) * 4;
        f32x4 c0 = *(const LDS_AS f32x4*)cp, c1 = *(const LDS_AS f32x4*)(cp + 16);
#pragma unroll
        for (int p = 0; p < 2; p++) {
          f32x4 st = *(const LDS_AS f32x4*)(lds + L_ST + (32 * p + l31) * 16);
          float v[8];
#pragma unroll
          for (int e = 0; e < 8; e++) v[e] = gelu_f(st[1] * (acc[p][8 * c + e] - st[0] * (e < 4 ? c0[e & 3] : c1[e & 3])));
          bst16((unsigned)(rt * T1728) + frag_off(q0 + 2 * c, 32 * p + l31), WS_H2,
                u32x4{cvtpk(v[0], v[1]), cvtpk(v[2], v[3]), cvtpk(v[4], v[5]), cvtpk(v[6], v[7])});
        }
      }
    }
    publish(IC<3>{});
    gemm(IC<4>{});
    exch();
    {
      int ln = lane; asm volatile("" : "+v"(ln));
      bf16_t* op = outp + (size_t)(rt * 64 + 32 * kh + (ln & 31)) * D_ + (cb * 10 + 4 * t + (ln >> 5)) * 8;
#pragma unroll
      for (int c = 0; c < 2; c++) {
        u32x4 o;
#pragma unroll
        for (int d = 0; d < 4; d++)
          o[d] = cvtpk(res(hr[c], 2 * d) + res(x1r[c], 2 * d) + acc[0][8 * c + 2 * d],
                       res(hr[c], 2 * d + 1) + res(x1r[c], 2 * d + 1) + acc[0][8 * c + 2 * d + 1]);
        *(u32x4*)(op + 16 * c) = o;
      }
    }
    CTS(19); CTS(20); CTS(21); CTS(22); CTS(23); CTS(27);
    if (w == 0) CTS_FLUSH(0);
    return;
  }

  const int a = w - 4, i = lane & 15, g = lane >> 4;
  const int row = 16 * a + i;
  bf16x8 G[59];
  const unsigned vG = cb * 102400 + a * 5120 + 4096 + lane * 16;
  const unsigned vG7 = cb * 276480 + a * 5120 + 4096 + lane * 16;
  const int b = a & 1;
  const unsigned vE = cb * 286720 + b * 14336 + (8 + a) * 1024 + lane * 16;
  const unsigned vO = cb * 286720 + (1 - b) * 14336 + (8 + a) * 1024 + lane * 16;
  const unsigned vS = cb * 286720 + b * 14336 + (12 + (a >> 1)) * 1024 + lane * 16;
  auto gl = [&](auto GI) {
    constexpr int n = decltype(GI)::value;
    if constexpr (GON == 0) { bf16x8 tmp_; asm volatile("" : "=v"(tmp_)); G[n] = tmp_; }
    else if constexpr (n < 5) G[n] = bload(vG, WS_WOT + n * 20480);
    else if constexpr (n < 10) G[n] = bload(vG, WS_W1T + (n - 5) * 20480);
    else if constexpr (n < 15) G[n] = bload(vG, WS_W2T + (n - 10) * 20480);
    else if constexpr (n < 45) {
      constexpr int c = (n - 15) / 6, k = (n - 15) % 6;
      if constexpr (k < 4) G[n] = bload((k & 1) ? vO : vE, WS_W3T + (4 * c + (k & ~1)) * 14336);
      else G[n] = bload(vS, WS_W3T + (4 * c + 2 * (k - 4)) * 14336);
    } else G[n] = bload(vG7, WS_W4T + (n - 45) * 20480);
  };
  const unsigned vT = rt * T640 + a * 1024 + lane * 16;
#define CH_GS {{9, 11, 12, 13, 15, 18}, {20, 22, 24, 26, 28, 31}, {33, 35, 37, 39, 41, 43}, {45, 48, 51, 55, 59, 59}}
  constexpr int GS[4][6] = CH_GS;
  constexpr int G2 = 7;
  u32x2 hr, x1r;
  hr = *(const u32x2*)(x + (size_t)(rt * 64 + row) * D_ + cb * 80 + 64 + 4 * g);
  sfor<0, 5>(gl);
  __builtin_amdgcn_sched_barrier(0);
  const unsigned so640 = rt * T640 + frag_off(cb * 10 + 8 + (g >> 1), row) + (g & 1) * 8;
  float mean1 = 0.f, r1 = 0.f;
  const char* aR[4];
#pragma unroll
  for (int p = 0; p < 4; p++) aR[p] = lds + a * 4096 + (a ^ p) * 1024 + lane * 16;
  const char* aSt[3];
#pragma unroll
  for (int d = 1; d < 4; d++) aSt[d - 1] = lds + (a ^ d) * 4096 + a * 1024 + lane * 16;
  gu32* flags = (gu32*)(ws + WS_FL);
  auto poll = [&](int ph) {
    int r2 = rt, wd = lane & 7;
    bool need = lane < 8;
    if (ph == 2) {
      if (lane >= 8 && lane < 16 && rt > 0 && (wd & 2) == 0) { r2 = rt - 1; need = true; }
      if (lane >= 16 && lane < 24 && rt < 31 && (wd & 2) != 0) { r2 = rt + 1; need = true; }
    }
    gu32* p = flags + (ph * 32 + r2) * 64 + wd;
#pragma unroll 1
    for (unsigned spins = 0; spins < (1u << 18); spins++) {
      unsigned v = need ? __hip_atomic_load(p, __ATOMIC_RELAXED, __HIP_MEMORY_SCOPE_AGENT) : 1u;
      if (__all(v == 1u)) break;
      __builtin_amdgcn_s_sleep(1);
    }
  };
  auto publish = [&](auto PH) {
    constexpr int ph = decltype(PH)::value;
    CTS(3 + 4 * ph);
    __builtin_amdgcn_sched_barrier(0);
    sfor<GS[ph][4], GS[ph][5]>(gl);
    waitvmc<GON * (GS[ph][5] - GS[ph][4])>();
    SBAR();
    CTS(4 + 4 * ph);
    if (tid == 256) __hip_atomic_store(flags + ((ph + 1) * 32 + rt) * 64 + cb, 1u, __ATOMIC_RELAXED, __HIP_MEMORY_SCOPE_AGENT);
  };
  auto stat_acc = [&](const bf16x8& f, float& s, float& q) {
    u32x4 u = __builtin_bit_cast(u32x4, f);
#pragma unroll
    for (int d = 0; d < 4; d++) { s = dot2(u[d], 0x3F803F80u, s); q = dot2(u[d], u[d], q); }
  };
  auto stat_fin = [&](float s, float q) {
    s = xsum4(s); q = xsum4(q);
    float mean = s * (1.0f / D_);
    float var = fmaxf(q * (1.0f / D_) - mean * mean, 0.f);
    float ra = __builtin_amdgcn_rsqf(var + 1e-5f);
    float rb = ra * __builtin_amdgcn_rsqf(var * ra * ra + 1e-5f);
    if (g == 0) *(LDS_AS f32x4*)(lds + L_ST + row * 16) = f32x4{mean, ra, rb, 0.f};
    mean1 = mean; r1 = ra;
    return rb;
  };
  f32x4 ac[4];
  auto exch = [&]() {
#pragma unroll
    for (int p = 1; p < 4; p++) *(LDS_AS f32x4*)(lds + LX_A + (a ^ p) * 3072 + (p - 1) * 1024 + lane * 16) = ac[p];
    LBAR();
#pragma unroll
    for (int p = 1; p < 4; p++) ac[0] += *(const LDS_AS f32x4*)(lds + LX_A + a * 3072 + (p - 1) * 1024 + lane * 16);
  };
  auto tile4 = [&](auto PH, float& ss, float& sq) {
    constexpr int ph = decltype(PH)::value;
    constexpr int G0 = ph == 0 ? G2 : GS[ph - 1][5], PC = 4;
#pragma unroll
    for (int p = 0; p < 4; p++) ac[p] = f32x4{0, 0, 0, 0};
    sfor<0, 5>([&](auto CI) {
      constexpr int c = decltype(CI)::value;
      constexpr int GP = c == 0 ? G0 : GS[ph][c - 1];
      waitbarv<PC * (4 - c) + GON * (GP - G0)>();
      bf16x8 f[4], wf;
#pragma unroll
      for (int p = 0; p < 4; p++) f[p] = ldsfrag(aR[p] + c * 16384);
      wf = G[ph * 5 + c];
      bf16x8 e[3];
      if constexpr (ph == 1) {
#pragma unroll
        for (int d = 0; d < 3; d++) e[d] = ldsfrag(aSt[d] + c * 16384);
      }
      sfor<GP, GS[ph][c]>(gl);
#pragma unroll
      for (int p = 0; p < 4; p++) ac[p] = mfma(wf, f[p], ac[p]);
      if constexpr (ph == 1) {
        stat_acc(f[0], ss, sq);
#pragma unroll
        for (int d = 0; d < 3; d++) stat_acc(e[d], ss, sq);
      }
      __builtin_amdgcn_sched_barrier(0);
    });
  };
  float ss = 0.f, sq = 0.f;
  p2();
  __builtin_amdgcn_sched_barrier(0);
  sfor<5, G2>(gl);
  waitvmc<GON * (G2 - 5)>();
  SBAR();
  CTS(26);
  if (tid == 256) __hip_atomic_store(flags + rt2 * 64 + cb2, 1u, __ATOMIC_RELAXED, __HIP_MEMORY_SCOPE_AGENT);
  poll(0);
  CTS(1);
  sfor<0, 20>([&](auto KI) { constexpr int ks = decltype(KI)::value; glds_sc1(ws + WS_O + ks * 4096, vT, ks * 4096 + a * 1024); });
  tile4(IC<0>{}, ss, sq);
  CTS(2);
  exch();
#pragma unroll
  for (int d = 0; d < 2; d++) hr[d] = cvtpk(bflo(hr[d]) + ac[0][2 * d], bfhi(hr[d]) + ac[0][2 * d + 1]);
  bst8(so640, WS_H, hr);
  publish(IC<0>{});
  poll(1);
  CTS(5);
  sfor<0, 20>([&](auto KI) { constexpr int ks = decltype(KI)::value; glds_sc1(ws + WS_H + ks * 4096, vT, ks * 4096 + a * 1024); });
  tile4(IC<1>{}, ss, sq);
  CTS(6);
  {
    float rs1 = stat_fin(ss, sq);
    exch();
    f32x4 c4 = *(const LDS_AS f32x4*)(lds + L_CS + (64 + 4 * g) * 4);
    float v[4];
#pragma unroll
    for (int e = 0; e < 4; e++) v[e] = gelu_f(rs1 * (ac[0][e] - mean1 * c4[e]));
    bst8(so640, WS_H1, u32x2{cvtpk(v[0], v[1]), cvtpk(v[2], v[3])});
  }
  publish(IC<1>{});
  {
    unsigned vSh[4];
#pragma unroll
    for (int k = 0; k < 4; k++) {
      const int sh = k == 0 ? 1 : k == 1 ? -1 : k == 2 ? 2 : -2;
      int r = row - sh + 64;
      vSh[k] = (unsigned)((rt + (r >> 6)) * T640 + ((r >> 4) & 3) * 1024 + (r & 15) * 16 + g * 256);
    }
    poll(2);
    CTS(9);
    sfor<0, 20>([&](auto KI) { constexpr int ks = decltype(KI)::value; glds_sc1(ws + WS_H1G + ks * 4096, vSh[ks / 5], ks * 4096 + a * 1024); });
  }
  tile4(IC<2>{}, ss, sq);
  CTS(10);
  exch();
#pragma unroll
  for (int d = 0; d < 2; d++)
    x1r[d] = cvtpk((bflo(hr[d]) - mean1) * r1 + ac[0][2 * d], (bfhi(hr[d]) - mean1) * r1 + ac[0][2 * d + 1]);
  bst8(so640, WS_X1, x1r);
  publish(IC<2>{});
  poll(3);
  CTS(13);
  sfor<0, 20>([&](auto KI) { constexpr int ks = decltype(KI)::value; glds_sc1(ws + WS_X1 + ks * 4096, vT, ks * 4096 + a * 1024); });
  {
    f32x4 ao[4], as[4];
#pragma unroll
    for (int u = 0; u < 4; u++) { ao[u] = f32x4{0, 0, 0, 0}; as[u] = f32x4{0, 0, 0, 0}; }
    const char *lE[4], *lO[4];
#pragma unroll
    for (int u = 0; u < 4; u++) {
      lE[u] = lds + b * 4096 + (u ^ a) * 1024 + lane * 16;
      lO[u] = lds + (1 - b) * 4096 + (u ^ a) * 1024 + lane * 16;
    }
    ss = 0.f; sq = 0.f;
    sfor<0, 5>([&](auto CI) {
      constexpr int c = decltype(CI)::value;
      constexpr int GP = c == 0 ? GS[2][5] : GS[3][c - 1];
      waitbarv<4 * (4 - c) + GON * (GP - GS[2][5])>();
      sfor<0, 4>([&](auto PI) {
        constexpr int p = decltype(PI)::value;
        bf16x8 f[4];
#pragma unroll
        for (int u = 0; u < 4; u++) f[u] = ldsfrag(((p & 1) ? lO[u] : lE[u]) + (4 * c + (p & ~1)) * 4096);
#pragma unroll
        for (int u = 0; u < 4; u++) ao[u] = mfma(G[15 + 6 * c + p], f[u], ao[u]);
        if constexpr ((p & 1) == 0) {
#pragma unroll
          for (int u = 0; u < 4; u++) as[u] = mfma(G[15 + 6 * c + 4 + (p >> 1)], f[u], as[u]);
        }
        stat_acc(f[0], ss, sq);
      });
      __builtin_amdgcn_sched_barrier(0);
      sfor<GP, GS[3][c]>(gl);
      __builtin_amdgcn_sched_barrier(0);
    });
    CTS(14);
    stat_fin(ss, sq);
    *(LDS_AS f32x4*)(lds + LX_A + (a ^ 1) * 3072 + lane * 16) = as[1];
    *(LDS_AS f32x4*)(lds + LX_A + (a ^ 1) * 3072 + 1024 + lane * 16) = as[3];
    LBAR();
    as[0] += *(const LDS_AS f32x4*)(lds + LX_A + a * 3072 + lane * 16);
    as[2] += *(const LDS_AS f32x4*)(lds + LX_A + a * 3072 + 1024 + lane * 16);
    int ln = lane; asm volatile("" : "+v"(ln));
    const int i = ln & 15, g = ln >> 4;
    const f32x4 co = *(const LDS_AS f32x4*)(lds + L_CS + 320 + (128 + 16 * a + 4 * g) * 4);
    const int st = 12 + (a >> 1);
    const bool shv = !(st == 13 && g >= 2);
    const f32x4 cs = *(const LDS_AS f32x4*)(lds + L_CS + 320 + (shv ? (16 * st + 4 * g) * 4 : 0));
    const int qo = cb * 27 + 16 + 2 * a + (g >> 1), qs = cb * 27 + 16 + 2 * (st - 8) + (g >> 1);
#pragma unroll
    for (int u = 0; u < 4; u++) {
      const int r = (u ^ a) * 16 + i;
      f32x4 s4 = *(const LDS_AS f32x4*)(lds + L_ST + r * 16);
      float v[4];
#pragma unroll
      for (int e = 0; e < 4; e++) v[e] = gelu_f(s4[1] * (ao[u][e] - s4[0] * co[e]));
      bst8((unsigned)(rt * T1728) + frag_off(qo, r) + (g & 1) * 8, WS_H2, u32x2{cvtpk(v[0], v[1]), cvtpk(v[2], v[3])});
      if ((u & 1) == 0) {
#pragma unroll
        for (int e = 0; e < 4; e++) v[e] = gelu_f(s4[1] * (as[u][e] - s4[0] * cs[e]));
        if (shv) bst8((unsigned)(rt * T1728) + frag_off(qs, r) + (g & 1) * 8, WS_H2, u32x2{cvtpk(v[0], v[1]), cvtpk(v[2], v[3])});
      }
    }
  }
  publish(IC<3>{});
  poll(4);
  CTS(17);
  {
    const unsigned vT7 = rt * T1728 + a * 1024 + lane * 16;
    auto issue = [&](auto CI) {
      constexpr int c = decltype(CI)::value;
      if constexpr (c < 14) {
        sfor<0, (c == 13 ? 2 : 4)>([&](auto KI) {
          constexpr int k = decltype(KI)::value;
          glds_sc1(ws + WS_H2 + (4 * c + k) * 4096, vT7, (c & 7) * 16384 + k * 4096 + a * 1024);
        });
      }
    };
    sfor<0, 6>(issue);
#pragma unroll
    for (int p = 0; p < 4; p++) ac[p] = f32x4{0, 0, 0, 0};
    sfor<0, 14>([&](auto CI) {
      constexpr int c = decltype(CI)::value;
      constexpr int last = c + 5 > 13 ? 13 : c + 5;
      constexpr int young = (last - c) * 4 - (last == 13 && c < 13 ? 2 : 0);
      waitbarv<young>();
      issue(IC<c + 6>{});
      if (c < 13 || a < 2) {
        bf16x8 f[4];
#pragma unroll
        for (int p = 0; p < 4; p++) f[p] = ldsfrag(aR[p] + (c & 7) * 16384);
#pragma unroll
        for (int p = 0; p < 4; p++) ac[p] = mfma(G[45 + c], f[p], ac[p]);
      }
      __builtin_amdgcn_sched_barrier(0);
    });
  }
  CTS(18);
  exch();
  {
    u32x2 o;
#pragma unroll
    for (int d = 0; d < 2; d++) o[d] = cvtpk(bflo(hr[d]) + bflo(x1r[d]) + ac[0][2 * d], bfhi(hr[d]) + bfhi(x1r[d]) + ac[0][2 * d + 1]);
    int ln = lane; asm volatile("" : "+v"(ln));
    *(u32x2*)(outp + (size_t)(rt * 64 + 16 * a + (ln & 15)) * D_ + cb * 80 + 64 + 4 * (ln >> 4)) = o;
  }
  CTS(19); CTS(20); CTS(21); CTS(22); CTS(23); CTS(27);
  if (w == 4) CTS_FLUSH(32);
}

extern "C" void kernel_launch(void* const* d_in, const int* in_sizes, int n_in, void* d_out, int out_size, void* d_ws,
                              size_t ws_size, hipStream_t stream) {
  const bf16_t* x = (const bf16_t*)d_in[0];
  const bf16_t* wqkv = (const bf16_t*)d_in[1];
  const bf16_t* wo = (const bf16_t*)d_in[2];
  const bf16_t* w1 = (const bf16_t*)d_in[4];
  const bf16_t* w2 = (const bf16_t*)d_in[5];
  const bf16_t* w3 = (const bf16_t*)d_in[6];
  const bf16_t* w4 = (const bf16_t*)d_in[7];
  char* ws = (char*)d_ws;
  bf16_t* out = (bf16_t*)d_out;
  (void)hipFuncSetAttribute((const void*)k_qkv, hipFuncAttributeMaxDynamicSharedMemorySize, K1_LDS);
  (void)hipFuncSetAttribute((const void*)k_chain, hipFuncAttributeMaxDynamicSharedMemorySize, CH_LDS);
  k_qkv<<<256, 512, K1_LDS, stream>>>(x, wqkv, wo, w1, w2, w3, w4, ws);
  k_chain<<<256, 512, CH_LDS, stream>>>(x, ws, out);
}
```

```cpp
#define K1_PFM 2
#define K1_PRE 3
#define K1_PTB 1
#define P2H 1
#define P2FG 5
#define P2FQ 64
#define POLL1 31
#define CPF 1
#define CPF_LEN 35000
#define CH_E 3
#define K1_SPF 1
#define K1_CPF 1
#define K1_OPF 3
#define K1_SPFW 0
#define K1_CPFW 1
#define OUTPF 7
#define OUTPP 2
#include <hip/hip_runtime.h>
#include <stdint.h>

typedef unsigned short bf16_t;
typedef __attribute__((ext_vector_type(8))) short bf16x8;
typedef __attribute__((ext_vector_type(4))) short s16x4;
typedef __attribute__((ext_vector_type(4))) float f32x4;
typedef __attribute__((ext_vector_type(2))) float f32x2;
typedef __attribute__((ext_vector_type(4))) unsigned u32x4;
typedef __attribute__((ext_vector_type(2))) unsigned u32x2;
#define LDS_AS __attribute__((address_space(3)))
#define GLB_AS __attribute__((address_space(1)))

constexpr int S_ = 2048, D_ = 640, F_ = 1728;

constexpr size_t MB_ = 1u << 20;
constexpr size_t WS_FL = 0, WS_REC = 65536, WS_CSP1 = 925696, WS_CSP3 = 976896,
                 WS_WOT = 1179648, WS_W1T = 1998848, WS_W2T = 2818048, WS_W3T = 3637248, WS_W4T = 5931008,
                 WS_PQ = 8142848, WS_O = 10764288, WS_H = 13385728,
                 WS_H1G = 16007168,
                 WS_H1 = WS_H1G + 81920, WS_X1 = 18792448, WS_H2 = 21413888;
constexpr int FL_BYTES = 65536;
constexpr int T640 = 81920, T1728 = 221184;
__device__ __forceinline__ unsigned frag_off(int q, int r) { return (unsigned)((q >> 2) * 4096 + (r >> 4) * 1024 + (q & 3) * 256 + (r & 15) * 16); }

#define STM_A 0
#define STM_X 0

template <int MODE> __device__ __forceinline__ void st16(void* p, u32x4 v) {
  if (MODE == 0) *(u32x4*)p = v;
  else if (MODE == 1) __builtin_nontemporal_store(v, (u32x4*)p);
  else if (MODE == 2) asm volatile("global_store_dwordx4 %0, %1, off sc0 sc1" ::"v"(p), "v"(v) : "memory");
  else asm volatile("global_store_dwordx4 %0, %1, off sc0 sc1 nt" ::"v"(p), "v"(v) : "memory");
}
template <int MODE> __device__ __forceinline__ void st8(void* p, u32x2 v) {
  if (MODE == 0) *(u32x2*)p = v;
  else if (MODE == 1) __builtin_nontemporal_store(v, (u32x2*)p);
  else if (MODE == 2) asm volatile("global_store_dwordx2 %0, %1, off sc0 sc1" ::"v"(p), "v"(v) : "memory");
  else asm volatile("global_store_dwordx2 %0, %1, off sc0 sc1 nt" ::"v"(p), "v"(v) : "memory");
}

__device__ __forceinline__ unsigned cvtpk(float lo, float hi) {
  unsigned r; asm("v_cvt_pk_bf16_f32 %0, %1, %2" : "=v"(r) : "v"(lo), "v"(hi)); return r;
}
__device__ __forceinline__ float bflo(unsigned v) { return __uint_as_float(v << 16); }
__device__ __forceinline__ float bfhi(unsigned v) { return __uint_as_float(v & 0xffff0000u); }
__device__ __forceinline__ void glds16(const void* g, char* l) {
  __builtin_amdgcn_global_load_lds((const GLB_AS void*)g, (LDS_AS void*)l, 16, 0, 0);
}
__device__ __forceinline__ f32x4 mfma(bf16x8 a, bf16x8 b, f32x4 c) {
  return __builtin_amdgcn_mfma_f32_16x16x32_bf16(a, b, c, 0, 0, 0);
}
__device__ __forceinline__ bf16x8 ldsfrag(const char* p) { return *(const LDS_AS bf16x8*)p; }
__device__ __forceinline__ s16x4 ldstr(const char* p) {
  typedef short v4i16_t __attribute__((ext_vector_type(4)));
  return __builtin_bit_cast(s16x4, __builtin_amdgcn_ds_read_tr16_b64_v4i16((LDS_AS v4i16_t*)p));
}
__device__ __forceinline__ float xsum4(float x) {
  auto s = __builtin_amdgcn_permlane16_swap(__float_as_uint(x), __float_as_uint(x), false, false);
  float y = __uint_as_float(s[0]) + __uint_as_float(s[1]);
  auto t = __builtin_amdgcn_permlane32_swap(__float_as_uint(y), __float_as_uint(y), false, false);
  return __uint_as_float(t[0]) + __uint_as_float(t[1]);
}
__device__ __forceinline__ float gelu_f(float x) {
  const float c0 = -2.302208198f;
  const float c1 = -0.1029432397f;
  float e = __builtin_amdgcn_exp2f(x * (c0 + c1 * x * x));
  return x * __builtin_amdgcn_rcpf(1.0f + e);
}
__device__ __forceinline__ float phi_f(float t) {
  float e = __builtin_amdgcn_exp2f(t * 1.4426950408889634f);
  return (t > 0.f ? t + 1.0f : e) + 1e-6f;
}
#define WAITBAR(N) asm volatile("s_waitcnt vmcnt(" #N ") lgkmcnt(0)\n\ts_barrier" ::: "memory")
#define LBAR() asm volatile("s_waitcnt lgkmcnt(0)\n\ts_barrier" ::: "memory")
template <int N> struct IC { static constexpr int value = N; };
#define TS_DECL
#define TS(k)
#define TS_FLUSH(kid, base)
#define CBAR() asm volatile("" ::: "memory")
template <int N> __device__ __forceinline__ void interleave_mfma_ds() {
#pragma unroll
  for (int k = 0; k < N; k++) { __builtin_amdgcn_sched_group_barrier(0x008, 1, 0); __builtin_amdgcn_sched_group_barrier(0x100, 1, 0); }
}

template <int I, int N, class F> __device__ __forceinline__ void sfor(F&& f) {
  if constexpr (I < N) { f(IC<I>{}); sfor<I + 1, N>(f); }
}
typedef __attribute__((ext_vector_type(2))) __bf16 bf16x2;
__device__ __forceinline__ float dot2(unsigned a, unsigned b, float c) {
  return __builtin_amdgcn_fdot2_f32_bf16(__builtin_bit_cast(bf16x2, a), __builtin_bit_cast(bf16x2, b), c, false);
}
__device__ __forceinline__ void glds_sc1(const char* sbase, unsigned voff, unsigned ldsoff) {
  asm volatile("s_mov_b32 m0, %2\n\tglobal_load_lds_dwordx4 %0, %1 sc1" ::"v"(voff), "s"(sbase), "s"(ldsoff) : "memory");
}
__device__ __forceinline__ void glds_pl(const char* sbase, unsigned voff, unsigned ldsoff) {
  asm volatile("s_mov_b32 m0, %2\n\tglobal_load_lds_dwordx4 %0, %1" ::"v"(voff), "s"(sbase), "s"(ldsoff) : "memory");
}
template <int N> __device__ __forceinline__ void waitvm() { asm volatile("s_waitcnt vmcnt(%0)" ::"n"(N) : "memory"); }
template <int N> __device__ __forceinline__ void waitbar() { asm volatile("s_waitcnt vmcnt(%0) lgkmcnt(0)\n\ts_barrier" ::"n"(N) : "memory"); }
#define SBAR() asm volatile("s_barrier" ::: "memory")

constexpr int PT_UNITS = 890;
constexpr int CSP3_KS = 8192, CSP3_CB = 256;
constexpr unsigned WS_DUM = 28u << 20;
struct PTS { const char* src; unsigned dst, csp; int ld, ks, cg, kind; };
__device__ __forceinline__ PTS pt_src(int u, const bf16_t* wo, const bf16_t* w1, const bf16_t* w2, const bf16_t* w3, const bf16_t* w4) {
  PTS s; int up, col0;
  s.ld = D_; s.kind = 0; s.csp = WS_DUM + u * 512; s.cg = 0;
  if (u < 570) {
    int nks = 20;
    if (u < 100) { s.src = (const char*)w1; s.dst = WS_W1T; up = u; }
    else if (u < 200) { s.src = (const char*)w2; s.dst = WS_W2T; up = u - 100; }
    else if (u < 300) { s.src = (const char*)wo; s.dst = WS_WOT; up = u - 200; }
    else { s.src = (const char*)w4; s.dst = WS_W4T; nks = 54; up = u - 300; }
    s.ks = up / 5; s.cg = up - s.ks * 5;
    col0 = s.cg * 128;
    if (u < 100) s.csp = WS_CSP1 + (s.ks * D_ + col0) * 4;
    s.dst += s.ks * 5120;
    s.cg |= nks << 8;
  } else {
    up = u - 570;
    s.ks = up >> 4; const int cb = (up >> 1) & 7; s.kind = 1 + (up & 1);
    s.src = (const char*)w3; s.ld = F_;
    col0 = cb * 216 + (up & 1) * 128;
    s.csp = WS_CSP3 + (s.ks * 8 + cb) * 1024 + (up & 1) * 512;
    s.dst = WS_W3T + (cb * 20 + s.ks) * 14336 + (up & 1) * 8192;
  }
  s.src += ((size_t)s.ks * 32 * s.ld + col0) * 2;
  return s;
}
#define STM_P 16
#define STM_Q 16

constexpr int K1_LDS = 5 * 32768;
#define K1_ZKV 1
#define K1_PF 1
#define K1_ORD 0
#define K1_PTNT 1
__device__ __forceinline__ void glds_nt(const char* sbase, unsigned voff, unsigned ldsoff) {
  asm volatile("s_mov_b32 m0, %2\n\tglobal_load_lds_dwordx4 %0, %1 nt" ::"v"(voff), "s"(sbase), "s"(ldsoff) : "memory");
}
#define K1_PT 1
__global__ __launch_bounds__(512, 1) void k_qkv(const bf16_t* __restrict__ x, const bf16_t* __restrict__ wqkv, const bf16_t* __restrict__ wo,
                                                const bf16_t* __restrict__ w1, const bf16_t* __restrict__ w2,
                                                const bf16_t* __restrict__ w3, const bf16_t* __restrict__ w4, char* __restrict__ ws) {
  extern __shared__ __attribute__((aligned(16))) char lds[];
  const int tid = threadIdx.x, lane = tid & 63;
  const int w = __builtin_amdgcn_readfirstlane(tid >> 6);
  const int xcd = blockIdx.x & 7, idx = blockIdx.x >> 3;
  const int rt = (xcd >> 1) * 4 + (idx >> 3), hp = (xcd & 1) * 8 + (idx & 7);
#define K1_HQ 1
  const int hq = hp ^ K1_HQ;
  const int i = lane & 15, g = lane >> 4, r = w & 3;
  const __amdgpu_buffer_rsrc_t rs = __builtin_amdgcn_make_buffer_rsrc(ws, 0, 0x10000000, 0x00020000);
  TS_DECL;
  TS(0);
  const unsigned voffA = (unsigned)((rt * 128 + 16 * w + (lane >> 3)) * 1280 + (((lane & 7) ^ (lane >> 3)) << 4));
  unsigned voffW;
  bool wld;
  {
    const int c = (lane & 15) ^ ((lane >> 4) << 1) ^ ((w & 1) << 3);
    const int col = c < 5 ? 8 * c + 40 * (hq - hp) : c <= 10 ? 640 + 8 * (c - 6) : c == 11 ? 1280 + 32 : 1280 + 8 * (c - 12);
    voffW = (unsigned)((8 * w + (lane >> 4)) * 3840 + (40 * hp + col) * 2);
    wld = c != 5;
  }
  auto G = [&](auto SI) {
    constexpr int st = decltype(SI)::value, sl = (st & 3) * 32768;
    glds_pl((const char*)x + st * 128, voffA, sl + w * 2048);
    glds_pl((const char*)x + st * 128 + 8 * 1280, voffA, sl + w * 2048 + 1024);
    if (wld) {
      glds_pl((const char*)wqkv + st * 64 * 3840, voffW, sl + 16384 + w * 2048);
      glds_pl((const char*)wqkv + st * 64 * 3840 + 4 * 3840, voffW, sl + 16384 + w * 2048 + 1024);
    }
  };
  constexpr int PFS[5][5] = {{0, 10, 10, 10, 10}, {1, 1, 10, 10, 10}, {1, 1, 4, 10, 10}, {1, 1, 3, 6, 10}, {0, 0, 0, 0, 0}};
  unsigned pfd[4] = {0, 0, 0, 0};
  auto PF = [&](auto KI) {
    constexpr int k = decltype(KI)::value, s0 = PFS[K1_PFM][k], s1 = PFS[K1_PFM][k + 1], n = (s1 - s0) * 46;
    if constexpr (K1_PF && n > 0) {
      const int it0 = lane * 8 + w, item = it0 < n ? it0 : n - 1;
      const int sq = item / 46, st = s0 + sq;
      const int rem = idx * 46 + item - sq * 46;
      const int r2 = rem - 512, row = r2 / 15, l = r2 - row * 15;
      const char* p = rem < 512 ? (const char*)x + (size_t)((xcd >> 1) * 512 + rem) * 1280 + st * 128
                                : (const char*)wqkv + (size_t)(st * 64 + row) * 3840 + (l / 5) * 1280 + (xcd & 1) * 640 + (l % 5) * 128;
      unsigned& d = pfd[k];
      asm volatile("global_load_dword %0, %1, off" : "=v"(d) : "v"(p) : "memory");
    }
  };
  constexpr int NPF[4] = {K1_PF && PFS[K1_PFM][1] > PFS[K1_PFM][0], K1_PF && PFS[K1_PFM][2] > PFS[K1_PFM][1],
                          K1_PF && PFS[K1_PFM][3] > PFS[K1_PFM][2], K1_PF && PFS[K1_PFM][4] > PFS[K1_PFM][3]};
  const bool hasu = K1_PT && w < 4 && blockIdx.x * 4 + r < PT_UNITS;
  PTS ps;
  auto ptload = [&]() {
    if (hasu) {
      int c0 = i ^ (g << 1), c1 = c0 ^ 8;
      if (ps.kind == 2) { c0 = c0 < 10 ? c0 : 10; c1 = c1 < 10 ? c1 : 10; }
      const unsigned v0 = (unsigned)(g * ps.ld * 2 + c0 * 16), v1 = (unsigned)(g * ps.ld * 2 + c1 * 16);
#pragma unroll
      for (int j = 0; j < 8; j++) {
        if (K1_PTNT) glds_nt(ps.src + (size_t)j * 8 * ps.ld, ((j >> 1) & 1) ? v1 : v0, 131072 + r * 8192 + j * 1024);
        else glds_pl(ps.src + (size_t)j * 8 * ps.ld, ((j >> 1) & 1) ? v1 : v0, 131072 + r * 8192 + j * 1024);
      }
    }
  };
#define K1_SPF_LEN 27200
#define K1_CPF_LEN 32000
  constexpr int PTB = K1_PRE < 4 && K1_PTB < 0 ? 0 : K1_PTB;
  auto GP = [&](auto SI) { PF(SI); G(SI); __builtin_amdgcn_sched_barrier(0); };
  unsigned cpd = 0;
  auto cpf = [&]() {
    if (w == K1_CPFW && idx < 5) {
      unsigned long long pc2;
      asm volatile("s_getpc_b64 s[20:21]\n\ts_add_u32 s20, s20, _Z7k_chainPKtPcPt@rel32@lo+4\n\ts_addc_u32 s21, s21, _Z7k_chainPKtPcPt@rel32@hi+12\n\ts_mov_b64 %0, s[20:21]"
                   : "=s"(pc2) :: "s20", "s21", "scc");
      const unsigned o = (unsigned)(idx * 8192 + lane * 128);
      asm volatile("global_load_dword %0, %1, %2" : "=v"(cpd) : "v"(o < K1_CPF_LEN ? o : K1_CPF_LEN), "s"(pc2) : "memory");
    }
  };
#define K1_CPOS 0
  auto warm = [&]() {
  if constexpr (K1_SPF) {
    if (w == K1_SPFW && idx < 4) {
      unsigned long long pc1;
      asm volatile("s_getpc_b64 s[20:21]\n\ts_add_u32 s20, s20, _Z5k_qkvPKtS0_S0_S0_S0_S0_S0_Pc@rel32@lo+4\n\ts_addc_u32 s21, s21, _Z5k_qkvPKtS0_S0_S0_S0_S0_S0_Pc@rel32@hi+12\n\ts_mov_b64 %0, s[20:21]"
                   : "=s"(pc1) :: "s20", "s21", "scc");
      const unsigned o = (unsigned)(idx * 8192 + lane * 128);
      asm volatile("global_load_dword %0, %1, %2" : "=v"(cpd) : "v"(o < K1_SPF_LEN ? o : K1_SPF_LEN), "s"(pc1) : "memory");
    }
    __builtin_amdgcn_sched_barrier(0);
  }
  if constexpr (K1_OPF == 1 || K1_OPF == 2) {
    if (w == 4 || (w == 5 && lane < 16)) {
      const unsigned o = (unsigned)((blockIdx.x * 80 + (w - 4) * 64 + lane) * 128);
      if (K1_OPF == 1) asm volatile("global_load_dword %0, %1, %2" : "=v"(cpd) : "v"(o), "s"(ws + WS_O) : "memory");
      else asm volatile("global_load_dword %0, %1, %2 nt" : "=v"(cpd) : "v"(o), "s"(ws + WS_O) : "memory");
    }
    __builtin_amdgcn_sched_barrier(0);
  }
  if constexpr (K1_CPF == 1) { cpf(); __builtin_amdgcn_sched_barrier(0); }
  };
  if constexpr (K1_CPOS == 0) warm();
  sfor<0, K1_PRE>(GP);
  if constexpr (K1_CPOS == 1) warm();
  if (hasu) ps = pt_src(blockIdx.x * 4 + r, wo, w1, w2, w3, w4);
  if (PTB < 0) ptload();
  if (!wld) {
    const u32x4 one = {0x3F803F80u, 0x3F803F80u, 0x3F803F80u, 0x3F803F80u};
#pragma unroll
    for (int k = 0; k < 8; k++) *(LDS_AS u32x4*)(lds + (k >> 1) * 32768 + 16384 + w * 2048 + (k & 1) * 1024 + lane * 16) = one;
  }
  int aoff[2];
#pragma unroll
  for (int m = 0; m < 2; m++) aoff[m] = ((2 * r + m) * 16 + i) * 128 + ((g ^ (i & 7)) << 4);
  float* tmean = (float*)(lds + 65536);
  float* trstd = tmean + 128;
  float* tcs = tmean + 256;
  char* red = lds + 65536 + 4096;
  const bf16x8 ones = {0x3F80, 0x3F80, 0x3F80, 0x3F80, 0x3F80, 0x3F80, 0x3F80, 0x3F80};
  const f32x4 z4 = {0, 0, 0, 0};

#define K1_GPOS 1
#define PT_LATE 1
#define K1_CSB 1
#define K1_ILV 2
  auto ilv = [&](auto NT_) {
    constexpr int NT = K1_CSB ? 5 : decltype(NT_)::value;
    if constexpr (K1_ILV == 1) interleave_mfma_ds<2 * NT>();
    else {
#pragma unroll
      for (int k = 0; k < NT + 1; k++) { __builtin_amdgcn_sched_group_barrier(0x008, 1, 0); __builtin_amdgcn_sched_group_barrier(0x100, K1_ILV, 0); }
    }
  };
#define K1_TLB 0
#define K1_TLBS 2
#define K1_TLBT 0
  auto tlbw = [&](unsigned& d) {
    if (w == 2 && idx < K1_TLB) {
      const int q = lane & 7, e = (lane >> 3) & 1;
      const unsigned t = xcd * 4 + 3 * e, ts = q == 3 ? 221184u : q >= 4 ? 12288u : 81920u;
      const unsigned b = q == 0 ? (unsigned)WS_H : q == 1 ? (unsigned)WS_H1 : q == 2 ? (unsigned)WS_X1 : q == 3 ? (unsigned)WS_H2 : (30u << 20) + (q & 1) * 393216u;
      if constexpr (K1_TLBT == 0) asm volatile("global_load_dword %0, %1, %2" : "=v"(d) : "v"(b + t * ts + e * (ts - 4)), "s"(ws) : "memory");
      else asm volatile("global_store_dword %0, %1, %2" :: "v"(b + t * ts + e * (ts - 4)), "v"(d), "s"(ws) : "memory");
    }
    __builtin_amdgcn_sched_barrier(0);
  };
  auto role = [&](auto ISQ_, auto HASU_) {
    constexpr bool ISQ = decltype(ISQ_)::value, HASU = decltype(HASU_)::value;
    constexpr int NT = ISQ ? 3 : 5, NT0 = ISQ ? 0 : 3;
    int woff[NT];
#pragma unroll
    for (int u = 0; u < NT; u++) {
      const int nt = NT0 + u, q = i >> 2, p = i & 3, f = q | ((g & 1) << 2);
      woff[u] = 16384 + (8 * g + q) * 256 + (((nt * 2 + (p >> 1)) ^ (f << 1)) << 4) + (p & 1) * 8;
    }
    constexpr int NX = K1_CSB && ISQ ? 2 : 1;
    int woffx[NX];
    if constexpr (K1_CSB && ISQ) {
#pragma unroll
      for (int u = 0; u < NX; u++) {
        const int nt = 2 * r + u, q = i >> 2, p = i & 3, f = q | ((g & 1) << 2);
        woffx[u] = 16384 + (8 * g + q) * 256 + (((nt * 2 + (p >> 1)) ^ (f << 1)) << 4) + (p & 1) * 8;
      }
    }
    f32x4 acc[2][NT], cs[NT], gr[2] = {z4, z4};
#pragma unroll
    for (int u = 0; u < NT; u++) { acc[0][u] = z4; acc[1][u] = z4; cs[u] = z4; }
    struct Frag { bf16x8 a[2], w[NT], x[NX]; };
    auto rd = [&](Frag& f, int slot, int kk) {
      const char* sl = lds + slot * 32768;
#pragma unroll
      for (int m = 0; m < 2; m++) f.a[m] = ldsfrag(sl + (aoff[m] ^ (kk * 64)));
#pragma unroll
      for (int u = 0; u < NT; u++) {
        s16x4 lo = ldstr(sl + woff[u] + kk * 8192), hi = ldstr(sl + woff[u] + kk * 8192 + 1024);
        f.w[u] = __builtin_shufflevector(lo, hi, 0, 1, 2, 3, 4, 5, 6, 7);
      }
      if constexpr (K1_CSB && ISQ) {
#pragma unroll
        for (int u = 0; u < NX; u++) {
          s16x4 lo = ldstr(sl + woffx[u] + kk * 8192), hi = ldstr(sl + woffx[u] + kk * 8192 + 1024);
          f.x[u] = __builtin_shufflevector(lo, hi, 0, 1, 2, 3, 4, 5, 6, 7);
        }
      }
    };
    auto mm = [&](const Frag& f) {
#pragma unroll
      for (int u = 0; u < NT; u++)
#pragma unroll
        for (int m = 0; m < 2; m++) acc[m][u] = ISQ ? mfma(f.w[u], f.a[m], acc[m][u]) : mfma(f.a[m], f.w[u], acc[m][u]);
      if constexpr (ISQ) { gr[0] = mfma(f.a[0], f.a[0], gr[0]); gr[1] = mfma(f.a[1], f.a[1], gr[1]); }
      if constexpr (K1_CSB && ISQ) { cs[0] = mfma(f.x[0], ones, cs[0]); cs[1] = mfma(f.x[1], ones, cs[1]); }
    };
    auto mcs = [&](const Frag& f, int sel) {
      if (!K1_CSB && r == sel) {
#pragma unroll
        for (int u = 0; u < NT; u++) cs[u] = ISQ ? mfma(f.w[u], ones, cs[u]) : mfma(ones, f.w[u], cs[u]);
      }
    };
    float cv[8];
    unsigned pa[8], po[8], pc[2];
    if constexpr (HASU) {
      const int q = i >> 2, p = i & 3, f = q | ((g & 1) << 2), pp = 2 * (p & 1) + (p >> 1);
      const unsigned sb = 131072 + r * 8192 + (8 * g + q) * 256;
      const int nb = (ps.cg >> 8) * 5120, cg8 = (ps.cg & 255) * 8;
      const unsigned vo32 = (unsigned)((g >> 1) * 1024 + (32 * (g & 1) + i) * 16), vo16 = (unsigned)(lane * 16);
#pragma unroll
      for (int j = 0; j < 8; j++) {
        const int Gi = cg8 + j, cb = Gi / 5, r5 = Gi - cb * 5;
        const bool n32 = ps.kind == 0 ? r5 < 4 : ps.kind == 1;
        const int jj = ps.kind == 2 && j > 5 ? 5 : j;
        const unsigned dst = ps.kind == 0 ? cb * nb + (r5 < 4 ? (r5 >> 1) * 2048 + (r5 & 1) * 256 : 4096) : ps.kind == 1 ? (j >> 1) * 2048 + (j & 1) * 256 : jj * 1024;
        const int pe = n32 ? pp : p;
        pa[j] = sb + (((jj * 2 + (pe >> 1)) ^ (f << 1)) << 4) + (pe & 1) * 8;
        po[j] = ps.dst + dst + (n32 ? vo32 : vo16);
        asm volatile("" : "+v"(pa[j]), "+v"(po[j]));
      }
#pragma unroll
      for (int h = 0; h < 2; h++) {
        const int Gj = 4 * h + g, Gi = cg8 + Gj;
        const bool n32 = ps.kind == 0 ? Gi - (Gi / 5) * 5 < 4 : ps.kind == 1;
        const int ci = n32 ? 8 * ((i >> 2) & 1) + 4 * (i >> 3) + (i & 3) : i;
        const int col = Gj * 16 + ci;
        const bool ok = !(ps.kind == 2 && col >= 88);
        pc[h] = ok ? ps.csp + (unsigned)col * 4 : WS_DUM + (blockIdx.x * 4 + r) * 512 + lane * 4;
        asm volatile("" : "+v"(pc[h]));
      }
    }
    auto pt_rd = [&](auto J, bf16x8& v) {
      constexpr int j = decltype(J)::value;
      s16x4 lo = ldstr(lds + pa[j]), hi = ldstr(lds + pa[j] + 1024);
      v = __builtin_shufflevector(lo, hi, 0, 1, 2, 3, 4, 5, 6, 7);
    };
    auto pt_st = [&](auto J, const bf16x8& v) {
      constexpr int j = decltype(J)::value;
      __builtin_amdgcn_raw_buffer_store_b128(__builtin_bit_cast(u32x4, v), rs, po[j], 0, STM_P);
      cv[j] = mfma(ones, v, z4)[0];
    };
    auto pt_cs = [&](auto H) {
      constexpr int h = decltype(H)::value;
      const float s = g == 0 ? cv[4 * h] : g == 1 ? cv[4 * h + 1] : g == 2 ? cv[4 * h + 2] : cv[4 * h + 3];
      __builtin_amdgcn_raw_buffer_store_b32(__float_as_uint(s), rs, pc[h], 0, STM_P);
    };
    bf16x8 pv0, pv1, pv2;
    Frag f0, f1;
    waitbar<4 * (K1_PRE - 1) + (K1_PRE > 1 ? NPF[1] : 0) + (K1_PRE > 2 ? NPF[2] : 0) + (K1_PRE > 3 ? NPF[3] : 0) + (HASU && PTB < 0 ? 8 : 0)>();
    TS(1);
    rd(f0, 0, 0);
    if constexpr (K1_PRE < 4) {
      sfor<K1_PRE, 4>(GP);
      __builtin_amdgcn_sched_barrier(0);
    }
    if constexpr (HASU && PTB == 0) { ptload(); __builtin_amdgcn_sched_barrier(0); }
    sfor<0, 10>([&](auto SI) {
      constexpr int s = decltype(SI)::value;
      rd(f1, s & 3, 1);
      mm(f0);
      ilv(IC<NT>{});
      __builtin_amdgcn_sched_barrier(0);
      mcs(f0, (2 * s) & 3);
      __builtin_amdgcn_sched_barrier(0);
      if constexpr (s < 9) {
        constexpr int b = s + 1;
        constexpr int n4 = HASU ? 2 : 0, n5 = HASU ? 3 : 0;
        constexpr int ypf = (b < 1 ? NPF[1] : 0) + (b < 2 ? NPF[2] : 0) + (b < 3 ? NPF[3] : 0);
        constexpr int ypt = HASU && b > PTB && b <= (PTB < 0 ? 3 : PTB + 3) ? 8 : 0;
        static_assert(PT_LATE && PTB <= 3, "");
        constexpr int yo = b <= 7 ? 8 + ypf + ypt : PT_LATE ? (b <= 7 ? 8 : b == 8 ? 4 + n5 : HASU ? 7 : 0)
                         : b == 4 ? 8 : b == 5 ? 8 + n4 : b == 6 ? 8 + n4 + n5 : b == 7 ? 8 + 2 * n4 + n5 : b == 8 ? 4 + 2 * n5 + n4 : n4 + n5;
        waitbar<yo>();
        if constexpr (b + 3 < 10 && (K1_GPOS == 0 || (K1_GPOS == 1 && ISQ))) G(IC<b + 3>{});
        if constexpr (ISQ && K1_TLB > 0 && K1_TLBS == b) tlbw(cpd);
        if constexpr (HASU && PTB == b) ptload();
        rd(f0, b & 3, 0);
        if constexpr (HASU && !PT_LATE && b >= 4 && b <= 7) { pt_rd(IC<2 * (b - 4)>{}, pv0); pt_rd(IC<2 * (b - 4) + 1>{}, pv1); }
        if constexpr (HASU && PT_LATE && b >= 7) { pt_rd(IC<3 * (b - 7)>{}, pv0); pt_rd(IC<3 * (b - 7) + 1>{}, pv1); if constexpr (b < 9) pt_rd(IC<3 * (b - 7) + 2>{}, pv2); }
      }
      mm(f1);
      if constexpr (s < 9) ilv(IC<NT>{});
      __builtin_amdgcn_sched_barrier(0);
      mcs(f1, (2 * s + 1) & 3);
      __builtin_amdgcn_sched_barrier(0);
      if constexpr (HASU && PT_LATE && s >= 6 && s <= 8) {
        pt_st(IC<3 * (s - 6)>{}, pv0); pt_st(IC<3 * (s - 6) + 1>{}, pv1);
        if constexpr (s < 8) pt_st(IC<3 * (s - 6) + 2>{}, pv2);
        if constexpr (s >= 7) pt_cs(IC<s - 7>{});
        __builtin_amdgcn_sched_barrier(0);
      }
      if constexpr (HASU && !PT_LATE && s >= 3 && s <= 6) {
        pt_st(IC<2 * (s - 3)>{}, pv0); pt_st(IC<2 * (s - 3) + 1>{}, pv1);
        if constexpr ((s & 1) == 0) pt_cs(IC<(s - 3) / 2>{});
        __builtin_amdgcn_sched_barrier(0);
      }
      if constexpr (s + 4 < 10 && ((K1_GPOS == 1 && !ISQ) || K1_GPOS == 2)) G(IC<s + 4>{});
      __builtin_amdgcn_sched_barrier(0);
    });
    asm volatile("" ::"v"(pfd[0]), "v"(pfd[1]), "v"(pfd[2]), "v"(pfd[3]), "v"(cpd));
    TS(2);
    unsigned opd = 0;
    if constexpr (ISQ && K1_OPF == 3) {
      if (w == 0 || (w == 1 && lane < 16))
        asm volatile("global_load_dword %0, %1, %2" : "=v"(opd) : "v"((unsigned)((blockIdx.x * 80 + w * 64 + lane) * 128)), "s"(ws + WS_O) : "memory");
      __builtin_amdgcn_sched_barrier(0);
    }
    if constexpr (ISQ && K1_OPF == 3 && K1_TLB > 0 && K1_TLBS == 10) tlbw(opd);
    if constexpr (ISQ) {
#pragma unroll
      for (int m = 0; m < 2; m++) {
        auto sw = __builtin_amdgcn_permlane32_swap(__float_as_uint(acc[m][2][0]), __float_as_uint(acc[m][2][0]), false, false);
        const float mean = __uint_as_float(sw[1]) * (1.0f / D_);
        const float q = (i & 3) == 0 ? gr[m][0] : (i & 3) == 1 ? gr[m][1] : (i & 3) == 2 ? gr[m][2] : gr[m][3];
        const float rstd = __builtin_amdgcn_rsqf(fmaxf(q * (1.0f / D_) - mean * mean, 0.f) + 1e-5f);
        if ((i >> 2) == g) { tmean[32 * r + 16 * m + i] = mean; trstd[32 * r + 16 * m + i] = rstd; }
      }
      if (i == 0) {
        if constexpr (K1_CSB) {
#pragma unroll
          for (int u = 0; u < 2; u++) *(f32x4*)(tcs + (2 * r + u) * 16 + 4 * g) = cs[u];
        } else {
#pragma unroll
          for (int u = 0; u < 3; u++) *(f32x4*)(tcs + r * 48 + u * 16 + 4 * g) = cs[u];
        }
      }
      LBAR();
      TS(3);
      f32x4 c4[3];
      float mean[2], rstd[2];
#pragma unroll
      for (int m = 0; m < 2; m++) { mean[m] = tmean[32 * r + 16 * m + i]; rstd[m] = trstd[32 * r + 16 * m + i]; }
#pragma unroll
      for (int u = 0; u < 3; u++) {
        const float* p = tcs + u * 16 + 4 * g;
        if constexpr (K1_CSB) c4[u] = *(const f32x4*)p;
        else c4[u] = (*(const f32x4*)p + *(const f32x4*)(p + 48)) + (*(const f32x4*)(p + 96) + *(const f32x4*)(p + 144));
      }
      unsigned pk[2][3][2];
#pragma unroll
      for (int m = 0; m < 2; m++)
#pragma unroll
        for (int u = 0; u < 3; u++) {
          float v[4];
#pragma unroll
          for (int e = 0; e < 4; e++) v[e] = phi_f(rstd[m] * (acc[m][u][e] - mean[m] * c4[u][e]));
          pk[m][u][0] = cvtpk(v[0], v[1]); pk[m][u][1] = cvtpk(v[2], v[3]);
        }
      const unsigned qb = (unsigned)(((rt * 2 + (r >> 1)) * 8 + (hq >> 1)) * 10240 + (hq & 1) * 5120 + ((r & 1) * 32 + i) * 16);
#pragma unroll
      for (int m = 0; m < 2; m++) {
        auto x0 = __builtin_amdgcn_permlane16_swap(pk[m][0][0], pk[m][1][0], false, false);
        auto x1 = __builtin_amdgcn_permlane16_swap(pk[m][0][1], pk[m][1][1], false, false);
        __builtin_amdgcn_raw_buffer_store_b128(u32x4{x0[0], x1[0], x0[1], x1[1]}, rs, qb + m * 256 + (2 * (g & 1) + (g >> 1)) * 1024, WS_PQ, STM_Q);
      }
      {
        auto x0 = __builtin_amdgcn_permlane16_swap(pk[0][2][0], pk[1][2][0], false, false);
        auto x1 = __builtin_amdgcn_permlane16_swap(pk[0][2][1], pk[1][2][1], false, false);
        if (g < 2) __builtin_amdgcn_raw_buffer_store_b128(u32x4{x0[0], x1[0], x0[1], x1[1]}, rs, qb + (g & 1) * 256 + 4 * 1024, WS_PQ, STM_Q);
      }
      LBAR();
      if constexpr (K1_OPF == 3) asm volatile("s_waitcnt vmcnt(0)" ::"v"(opd) : "memory");
    } else {
      {
        const int t = (blockIdx.x * 4 + r) * 64 + lane;
        if (t < FL_BYTES / 16) __builtin_amdgcn_raw_buffer_store_b128(u32x4{0, 0, 0, 0}, rs, t * 16, WS_FL, STM_Q);
        else if (t < FL_BYTES / 16 + 34 * 320) {
          const int z = t - FL_BYTES / 16, tl = z / 320, z2 = z - tl * 320, q = z2 >> 2, k = z2 & 3;
          const unsigned v = tl == 0 || tl == 33 ? 0u : ~0u;
          __builtin_amdgcn_raw_buffer_store_b128(u32x4{v, v, v, v}, rs, tl * T640 + frag_off(q, k < 2 ? k : 60 + k), WS_H1G, STM_Q);
        }
      }
      if (!K1_CSB && g == 0) {
#pragma unroll
        for (int u = 0; u < 5; u++) tcs[192 + r * 80 + u * 16 + i] = cs[u][0];
      }
      LBAR();
      TS(3);
      bf16x8 op[5];
      f32x4 m4[2], r4[2];
#pragma unroll
      for (int m = 0; m < 2; m++) { m4[m] = *(const f32x4*)(tmean + 32 * r + 16 * m + 4 * g); r4[m] = *(const f32x4*)(trstd + 32 * r + 16 * m + 4 * g); }
#pragma unroll
      for (int u = 0; u < 5; u++) {
        const float* p = tcs + (K1_CSB ? 48 : 192) + u * 16 + i;
        const float c = K1_CSB ? p[0] : (p[0] + p[80]) + (p[160] + p[240]);
        u32x4 o;
#pragma unroll
        for (int m = 0; m < 2; m++) {
          float v[4];
#pragma unroll
          for (int e = 0; e < 4; e++) {
            float a = r4[m][e] * (acc[m][u][e] - m4[m][e] * c);
            v[e] = u < 2 ? phi_f(a) : u == 2 ? (i < 8 ? phi_f(a) : a) : a;
          }
          o[2 * m] = cvtpk(v[0], v[1]); o[2 * m + 1] = cvtpk(v[2], v[3]);
        }
        op[u] = __builtin_bit_cast(bf16x8, o);
      }
      char* rw = red + r * 10240 + lane * 16;
      *(f32x4*)(rw + 0 * 1024) = mfma(op[0], op[3], z4);
      *(f32x4*)(rw + 1 * 1024) = mfma(op[0], op[4], z4);
      *(f32x4*)(rw + 2 * 1024) = mfma(op[1], op[3], z4);
      *(f32x4*)(rw + 3 * 1024) = mfma(op[1], op[4], z4);
      *(f32x4*)(rw + 4 * 1024) = mfma(op[1], op[2], z4);
      *(f32x4*)(rw + 5 * 1024) = mfma(op[2], op[4], z4);
      *(f32x4*)(rw + 6 * 1024) = mfma(op[2], op[2], z4);
      *(f32x4*)(rw + 7 * 1024) = mfma(op[0], ones, z4);
      *(f32x4*)(rw + 8 * 1024) = mfma(op[1], ones, z4);
      *(f32x4*)(rw + 9 * 1024) = mfma(op[2], ones, z4);
      LBAR();
      const int t = tid - 256;
      if (t < 210) {
        const int h = t >= 105, rem = t - 105 * h;
        const bool isz = rem >= 100;
        const int e = isz ? 0 : rem / 5, dq = isz ? rem - 100 : rem - e * 5;
        int dt, gg, et, ii;
        if (h == 0) { dt = dq < 4 ? 0 : 1; gg = dq < 4 ? dq : 0; et = e < 16 ? 0 : 1; ii = e & 15; }
        else { dt = dq < 3 ? 1 : 2; gg = dq < 3 ? dq + 1 : dq - 3; et = e < 12 ? 1 : 2; ii = e < 12 ? e + 4 : e - 4; }
        const int tile = isz ? 7 + dt : dt == 0 ? et : dt == 1 ? 2 + et : 4 + et;
        const char* p = red + tile * 1024 + (gg * 16 + (isz ? 0 : ii)) * 16;
        f32x4 s = *(const f32x4*)p;
#pragma unroll
        for (int k = 1; k < 4; k++) s += *(const f32x4*)(p + k * 10240);
        __builtin_amdgcn_raw_buffer_store_b128(__builtin_bit_cast(u32x4, s), rs, (unsigned)(((rt * 16 + hp) * 210 + t) * 16), WS_REC, STM_Q);
      }
    }
    TS(5);
  };
  if (w >= 4) role(IC<0>{}, IC<0>{});
  else if (hasu) role(IC<1>{}, IC<K1_PT>{});
  else role(IC<1>{}, IC<0>{});
  if (w == 0) TS_FLUSH(1, 0);
  if (w == 4) TS_FLUSH(1, 8);
}

typedef __attribute__((ext_vector_type(16))) float f32x16;
typedef GLB_AS unsigned gu32;
constexpr int LX_M = 131072, LX_A = 147456, L_ST = 159744, L_CS = 160768, L_T = 131072, L_FL = 162048, CH_LDS = 162560 + (OUTPP == 3 ? 256 : 0);
#define CH_PFU 0
#define CH_PFB WS_W1T
#define CH_PFK {0, 0, 0, 0, 0}
#define CH_PFS 15
#define XEARLY 0
#define HALO 1
#define FL1 1
#define POLLM 0
#define POLL_NC 2
#define POLL_SL 0
#define CH_RD 28
#define CH_QZ 4
#define OUT_ST 0
#define P2_EARLYBAR 0
#define P2_LATEW 1
#define P2_NP0 4
#define P2_A0 8
#define P2_A1 14
#define P2_B0 3
#define P2_B1 5
#define P2_B2 2
#define CH_E6 CH_E
#define CH_E7 6
#define P7X 0
constexpr int hi7(int c) {
  int h = CH_E7 - 1 + 2 * (c + 1);
  if (h > c + 6) h = c + 6;
  return h > 13 ? 13 : h;
}
#define OUT_ROW (OUTPF == 3 || OUTPF == 6 ? (lane & 1) * 63 : OUTPF == 4 ? (lane & 3) * 21 : lane)
#define OUT_COL (OUTPF >= 4 ? ((cb + 4) & 7) * 160 : cb * 160 + (a - 1) * 128)
#define OUT_LDS (OUTPP == 3 ? 162560 : 98304 + w * 256)
#define OPF 0
#define OPFW 6
#define DF 0
#define DF_DLY 4
constexpr int young5(int E, int c, int n0, int n1, int n2, int n3) {
  const int n[4] = {n0, n1, n2, n3};
  int y = 0;
  if (c < E) { y = (E - 1 - c) * 4; for (int k = 0; k < c; k++) y += (k + E < 5 ? 4 : 0) + n[k]; }
  else { y = n[c - E]; for (int k = c - E + 1; k < c; k++) y += (k + E < 5 ? 4 : 0) + n[k]; }
  return y;
}
#define AUXP 0
#define AUXP6 1
#define AUXP7 2
#define PSTAT 1
constexpr unsigned WS_ST = 30u << 20;
constexpr int L_SP = 81920;
constexpr int RD = CH_RD, NFR = 154, QZ = CH_QZ;
constexpr int PBND[6] = {0, 20, 40, 60, 100, 154};
#define CH_NPRE 4
#define CH_SL 1
#define CH_DEN 2
#define CH_T2 88
constexpr int NPRE = CH_NPRE;
#define CH_T3G 128
#define CH_E3 0
struct LSched { int step[NFR]; int epi[5]; int pre[5]; int pub[5]; int pre2, pub2; };
constexpr LSched make_sched() {
  LSched s{};
  s.pre2 = 20 + NPRE; s.pub2 = RD < 40 ? RD : 40;
  int issued = s.pub2;
  constexpr int TG[5] = {40, 60, CH_T2, 128, 154};
  for (int p = 0; p < 5; p++) {
    for (int f = PBND[p]; f < PBND[p + 1]; f++) {
      const int den = p < 3 ? CH_DEN : 1;
      bool go = (f - PBND[p]) % den == 0 && issued < (p == 3 ? CH_T3G : TG[p]) && issued < f + 1 + RD;
      if (issued >= PBND[p + 1] && f >= PBND[p + 1] - QZ) go = false;
      if (issued < PBND[p + 1] && issued <= f + 4) go = true;
      if (go) issued++;
      s.step[f] = issued;
    }
    int lim = PBND[p + 1] + RD; if (lim > TG[p]) lim = TG[p];
    if (lim < issued) lim = issued;
    if (p == 3) issued = issued + 4 * CH_E3 < lim ? issued + 4 * CH_E3 : lim;
    s.epi[p] = issued;
    s.pre[p] = issued + NPRE < lim ? issued + NPRE : lim;
    issued = lim;
    s.pub[p] = issued;
  }
  return s;
}
constexpr LSched LS = make_sched();
constexpr int MON = 1;
constexpr int GON = 1;

__device__ __forceinline__ f32x16 mfma32(bf16x8 a, bf16x8 b, f32x16 c) {
  return __builtin_amdgcn_mfma_f32_32x32x16_bf16(a, b, c, 0, 0, 0);
}
template <int N> __device__ __forceinline__ void waitvmc() { __builtin_amdgcn_s_waitcnt((N & 15) | ((N >> 4) << 14) | 0x0F70); }
__device__ __forceinline__ void glds_pf(const char* sbase, unsigned voff, unsigned ldsoff) {
  asm volatile("s_mov_b32 m0, %2\n\tglobal_load_lds_dword %0, %1" ::"v"(voff), "s"(sbase), "s"(ldsoff) : "memory");
}
__device__ __forceinline__ void glds_pf1(const char* sbase, unsigned voff, unsigned ldsoff) {
  asm volatile("s_mov_b32 m0, %2\n\tglobal_load_lds_dword %0, %1 sc1" ::"v"(voff), "s"(sbase), "s"(ldsoff) : "memory");
}
template <int N> __device__ __forceinline__ void waitbarv() { asm volatile("s_waitcnt vmcnt(%0)\n\ts_barrier" ::"n"(N) : "memory"); }
#define VM0() __builtin_amdgcn_s_waitcnt(0x0F70)
#define CTSP2(k) CTS(k)
#define CTSP6(k)
#define CTS_DECL
#define CTS(k)
#define CTS_FLUSH(base)

__global__ __launch_bounds__(512, 1) void k_chain(const bf16_t* __restrict__ x, char* __restrict__ ws, bf16_t* __restrict__ outp) {
  extern __shared__ __attribute__((aligned(16))) char lds[];
  const int tid = threadIdx.x, lane = tid & 63;
  const int w = __builtin_amdgcn_readfirstlane(tid >> 6);
  const int xcd = blockIdx.x & 7, idx = blockIdx.x >> 3;
  const int rt = xcd * 4 + (idx >> 3), cb = idx & 7;
#define P2MAP 1
  const int rt2 = P2MAP ? idx : rt, cb2 = P2MAP ? xcd : cb;
  const __amdgpu_buffer_rsrc_t rs = __builtin_amdgcn_make_buffer_rsrc(ws, 0, 0x10000000, 0x00020000);
  auto bload = [&](unsigned v, unsigned so) { return __builtin_bit_cast(bf16x8, __builtin_amdgcn_raw_buffer_load_b128(rs, v, so, 0)); };
  auto bst16 = [&](unsigned v, unsigned so, u32x4 d) { __builtin_amdgcn_raw_buffer_store_b128(d, rs, v, so, 16); };
  auto bst8 = [&](unsigned v, unsigned so, u32x2 d) { __builtin_amdgcn_raw_buffer_store_b64(d, rs, v, so, 16); };
  CTS_DECL;
  CTS(0);

  const int hl = idx & 3, h2 = xcd * 4 + hl, rg = idx >> 2;
  f32x4 rv[4], cv[4];
  bf16x8 bq[2];
  {
    const int pc = lane & 15, pg = lane >> 4;
    int piece = 14 * w + (pc < 14 ? pc : 0);
    if (piece > 104) piece = 104;
    const unsigned rb = (unsigned)WS_REC + (unsigned)(h2 * 1680 + piece * 16 + pg * (4 * 53760));
    CTSP2(20);
#pragma unroll
    for (int p = 0; p < 4; p++) rv[p] = __builtin_bit_cast(f32x4, __builtin_amdgcn_raw_buffer_load_b128(rs, rb + p * 53760, 0, 0));
    const int c0 = (hl * 5) >> 1, c = c0 + pg < 9 ? c0 + pg : 9;
#pragma unroll
    for (int m = 0; m < 2; m++)
      bq[m] = bload((unsigned)(((4 * rg + (w >> 1)) * 8 + xcd) * 10240 + c * 1024 + ((w & 1) * 32 + 16 * m + pc) * 16), WS_PQ);
    CBAR();
    if (w == 7) {
      const char* pcv; asm volatile("s_getpc_b64 %0" : "=s"(pcv));
      const unsigned o = (unsigned)((idx % 5) * 8192 + lane * 128);
      glds_pf(pcv, o < CPF_LEN ? o : CPF_LEN, 98304);
    }
#define TLBC 0
    CTSP2(21);
    if (tid < 63) {
      const int n = tid / 3, j = tid - 3 * n;
      *(LDS_AS u32x2*)(lds + L_T + n * 80 + ((hl & 1) && j == 0 ? 0 : 5 + j) * 8) = u32x2{0, 0};
    }
  }
  auto p2 = [&](auto&& h0, auto&& h1) {
    const int i = lane & 15, g = lane >> 4;
    __builtin_amdgcn_sched_barrier(0);
    h0();
    {
      const int grp = tid / 74, qd = tid - 74 * grp;
      const unsigned cst = qd < 20 ? 2560u : (unsigned)CSP3_KS;
      unsigned cbs = (qd < 20 ? (unsigned)WS_CSP1 + (cb * 80 + 4 * qd) * 4 : (unsigned)WS_CSP3 + (cb * CSP3_CB + 4 * (qd - 20)) * 4) + grp * 4 * cst;
      if (tid >= 370) cbs = 0x7000000u * 4u;
#pragma unroll
      for (int p = 0; p < 4; p++) cv[p] = __builtin_bit_cast(f32x4, __builtin_amdgcn_raw_buffer_load_b128(rs, cbs + p * cst, 0, 0));
    }
    __builtin_amdgcn_sched_barrier(0);
    f32x4 s = (rv[0] + rv[1]) + (rv[2] + rv[3]);
#pragma unroll
    for (int k = 0; k < 4; k++) s[k] = xsum4(s[k]);
    CTSP2(24);
    {
      const int piece = 14 * w + i, e = piece / 5, dq = piece - 5 * e;
      if (g == 0 && i < 14 && piece < 105)
        *(LDS_AS u32x2*)(lds + L_T + e * 80 + ((hl & 1) + dq) * 8) = u32x2{cvtpk(s[0], s[1]), cvtpk(s[2], s[3])};
    }
    __builtin_amdgcn_sched_barrier(0);
    h1();
    __builtin_amdgcn_sched_barrier(0);
    LBAR();
    const bf16x8 a0 = ldsfrag(lds + L_T + i * 80 + g * 16), a1 = ldsfrag(lds + L_T + (i < 4 ? 16 + i : 20) * 80 + g * 16);
    const f32x4 z4 = {0, 0, 0, 0};
    f32x4 n0[2], n1[2];
#pragma unroll
    for (int m = 0; m < 2; m++) { n1[m] = mfma(a1, bq[m], z4); n0[m] = mfma(a0, bq[m], z4); }
    const int q4 = 5 * h2 + g, q5 = 5 * h2 + 4;
    const unsigned ob = (unsigned)((4 * rg + (w >> 1)) * T640);
#pragma unroll
    for (int m = 0; m < 2; m++) {
      auto sw = __builtin_amdgcn_permlane16_swap(__float_as_uint(n1[m][0]), __float_as_uint(n1[m][0]), false, false);
      const float inv = __builtin_amdgcn_rcpf(__uint_as_float(sw[1]) + 1e-6f);
      const int r = (w & 1) * 32 + 16 * m + i;
      bst8(ob + frag_off(q4 >> 1, r) + (q4 & 1) * 8, WS_O, u32x2{cvtpk(n0[m][0] * inv, n0[m][1] * inv), cvtpk(n0[m][2] * inv, n0[m][3] * inv)});
      if (g == 0) bst8(ob + frag_off(q5 >> 1, r) + (q5 & 1) * 8, WS_O, u32x2{cvtpk(n1[m][0] * inv, n1[m][1] * inv), cvtpk(n1[m][2] * inv, n1[m][3] * inv)});
    }
    __builtin_amdgcn_sched_barrier(0);
    if (tid < 370) *(LDS_AS f32x4*)(lds + L_SP + tid * 16) = (cv[0] + cv[1]) + (cv[2] + cv[3]);
    __builtin_amdgcn_s_waitcnt(0xC07F);
    CTSP2(25);
  };

  if (w < 4) {
    const int t = w & 1, kh = w >> 1, hh = lane >> 5, l31 = lane & 31;
    const unsigned vA = cb * 102400 + kh * 5120 + t * 2048 + lane * 16;
    const unsigned vB = cb * 286720 + w * 2048 + lane * 16;
    const unsigned vC = cb * 276480 + kh * 5120 + t * 2048 + lane * 16;
    bf16x8 ring[RD];
    auto wl = [&](auto FI) {
      constexpr int F = decltype(FI)::value;
      constexpr int ph = F < 20 ? 0 : F < 40 ? 1 : F < 60 ? 2 : F < 100 ? 3 : 4;
      constexpr int f = F - PBND[ph];
      constexpr unsigned so = ph == 0 ? WS_WOT + (f >> 1) * 10240 : ph == 1 ? WS_W1T + (f >> 1) * 10240 : ph == 2 ? WS_W2T + (f >> 1) * 10240
                            : ph == 3 ? WS_W3T + (f >> 1) * 14336 : WS_W4T + (f >> 1) * 10240;
      ring[F % RD] = bload((ph < 3 ? vA : ph == 3 ? vB : vC) + (f & 1) * 1024, so);
    };
    const int row = 32 * kh + l31;
    const int colq = cb * 10 + 4 * t + hh;
    u32x4 hr[2], x1r[2];
    auto ldres = [&]() {
      const bf16_t* xp = x + (size_t)(rt * 64 + row) * D_ + colq * 8;
      hr[0] = *(const u32x4*)xp; hr[1] = *(const u32x4*)(xp + 16);
    };
    const unsigned so640[2] = {(unsigned)(rt * T640) + frag_off(colq, row), (unsigned)(rt * T640) + frag_off(colq + 2, row)};
    const unsigned lp = ((lane >> 4) & 1) * 1024 + hh * 256 + (lane & 15) * 16;
    const char* aA[2] = {lds + kh * 4096 + kh * 2048 + lp, lds + kh * 4096 + (kh ^ 1) * 2048 + lp};
    const char* aL = lds + lp;
    f32x16 acc[2];
    bf16x8 af[3][2];
    float mean1 = 0.f, r1 = 0.f;

    constexpr int PFK[5] = CH_PFK;
    const int pfu = (blockIdx.x >> 3) * 4 + w;
    auto gemm = [&](auto PH) {
      constexpr int ph = decltype(PH)::value;
      constexpr int F0 = PBND[ph], NS = PBND[ph + 1] - F0;
      auto rdA = [&](auto SI) {
        constexpr int s = decltype(SI)::value;
        if constexpr (ph == 3) {
          constexpr int o = (s >> 1) * 4096 + (s & 1) * 512;
          af[s % 3][0] = ldsfrag(aL + o); af[s % 3][1] = ldsfrag(aL + o + 2048);
        } else {
          constexpr int o = (ph == 4 ? ((s >> 2) & 7) * 16384 + ((s >> 1) & 1) * 8192 : (s >> 1) * 8192) + (s & 1) * 512;
          af[s % 3][0] = ldsfrag(aA[0] + o); af[s % 3][1] = ldsfrag(aA[1] + o);
        }
      };
#pragma unroll
      for (int e = 0; e < 16; e++) { acc[0][e] = 0.f; acc[1][e] = 0.f; }
      if constexpr (((POLL1 >> ph) & 1) && !(ph > 0 && ((DF >> (ph > 0 ? ph - 1 : 0)) & 1))) SBAR();
      SBAR();
      CTS(1 + 4 * ph);
      rdA(IC<0>{}); rdA(IC<1>{});
      __builtin_amdgcn_sched_barrier(0);
      sfor<0, NS>([&](auto SI) {
        constexpr int s = decltype(SI)::value;
        if constexpr (s + 2 < NS) {
          if constexpr (((s + 2) & (ph == 3 ? 7 : 3)) == 0) SBAR();
          if constexpr (ph == 3 && ((s + 2) & 7) == 0) { constexpr int q_ = (s + 2) >> 3; CTSP6(q_ == 1 ? 24 : q_ == 2 ? 25 : q_ == 3 ? 26 : 20); }
          rdA(IC<s + 2>{});
        }
        acc[0] = mfma32(ring[(F0 + s) % RD], af[s % 3][0], acc[0]);
        acc[1] = mfma32(ring[(F0 + s) % RD], af[s % 3][1], acc[1]);
        constexpr int l0 = (F0 + s) == 0 ? LS.pub2 : (s == 0 ? LS.pub[ph > 0 ? ph - 1 : 0] : LS.step[F0 + s - (F0 + s > 0 ? 1 : 0)]);
        sfor<l0, LS.step[F0 + s]>(wl);
        if constexpr (s == CH_PFS && PFK[ph] > 0) {
          constexpr int base = (ph > 0 ? PFK[0] : 0) + (ph > 1 ? PFK[1] : 0) + (ph > 2 ? PFK[2] : 0) + (ph > 3 ? PFK[3] : 0);
#pragma unroll
          for (int r = 0; r < PFK[ph]; r++)
            if ((base + r) * 128 + pfu < 550) glds_pf(ws + WS_W3T + (base + r) * 1048576, (unsigned)(pfu * 8192 + lane * 128), 98304 + w * 256);
        }
        __builtin_amdgcn_sched_barrier(0);
      });
      CTS(2 + 4 * ph);
    };
    auto exch = [&]() {
      char* xw = lds + LX_M + w * 4096 + lane * 16;
      const char* xr = lds + LX_M + (w ^ 2) * 4096 + lane * 16;
#pragma unroll
      for (int j = 0; j < 4; j++) *(LDS_AS f32x4*)(xw + j * 1024) = f32x4{acc[1][4 * j], acc[1][4 * j + 1], acc[1][4 * j + 2], acc[1][4 * j + 3]};
      LBAR();
#pragma unroll
      for (int j = 0; j < 4; j++) {
        f32x4 v = *(const LDS_AS f32x4*)(xr + j * 1024);
#pragma unroll
        for (int e = 0; e < 4; e++) acc[0][4 * j + e] += v[e];
      }
    };
    auto wlp = [&](auto FI) { wl(FI); __builtin_amdgcn_s_sleep(CH_SL); __builtin_amdgcn_sched_barrier(0); };
    auto publish = [&](auto PH) {
      constexpr int ph = decltype(PH)::value;
      CTS(3 + 4 * ph);
      __builtin_amdgcn_sched_barrier(0);
      sfor<LS.epi[ph], LS.pre[ph]>(wlp);
      if constexpr (!((DF >> ph) & 1)) {
        waitvmc<MON * (LS.pre[ph] - LS.epi[ph])>();
        SBAR();
      }
      CTS(4 + 4 * ph);
      sfor<LS.pre[ph], LS.pub[ph]>(wlp);
      __builtin_amdgcn_sched_barrier(0);
    };
    auto res = [&](const u32x4& r, int e) { return (e & 1) ? bfhi(r[e >> 1]) : bflo(r[e >> 1]); };
    const unsigned vSP = (unsigned)(((rt * 24 + 3 * cb + t) * 64 + row) * 8);
    auto pubstat = [&](int k, float sm, float sqv) {
      auto a0 = __builtin_amdgcn_permlane32_swap(__float_as_uint(sm), __float_as_uint(sm), false, false);
      auto a1 = __builtin_amdgcn_permlane32_swap(__float_as_uint(sqv), __float_as_uint(sqv), false, false);
      const float s2 = __uint_as_float(a0[0]) + __uint_as_float(a0[1]), q2 = __uint_as_float(a1[0]) + __uint_as_float(a1[1]);
      if (hh == 0) bst8(vSP, WS_ST + k * 393216, u32x2{__float_as_uint(s2), __float_as_uint(q2)});
    };

    p2([&]() { sfor<0, P2_A0>(wl); }, [&]() { sfor<P2_A0, P2_A1>(wl); });
    __builtin_amdgcn_sched_barrier(0);
    sfor<P2_A1, P2_A1 + P2_NP0>(wlp);
    waitvmc<MON * P2_NP0>();
    SBAR();
    CTSP2(26);
    sfor<P2_A1 + P2_NP0, 20>(wl);
    ldres();
    __builtin_amdgcn_sched_barrier(0);
    sfor<20, LS.pub2>(wlp);
    __builtin_amdgcn_sched_barrier(0);
    gemm(IC<0>{});
    exch();
    {
      float ps[2] = {0.f, 0.f}, pq[2] = {0.f, 0.f};
#pragma unroll
      for (int c = 0; c < 2; c++) {
#pragma unroll
        for (int d = 0; d < 4; d++) {
          const float v0 = res(hr[c], 2 * d) + acc[0][8 * c + 2 * d], v1 = res(hr[c], 2 * d + 1) + acc[0][8 * c + 2 * d + 1];
          hr[c][d] = cvtpk(v0, v1);
          if (PSTAT) { ps[0] += v0; ps[1] += v1; pq[0] = fmaf(v0, v0, pq[0]); pq[1] = fmaf(v1, v1, pq[1]); }
        }
        bst16(so640[c], WS_H, hr[c]);
      }
      if (PSTAT) pubstat(0, ps[0] + ps[1], pq[0] + pq[1]);
    }
    publish(IC<0>{});
    gemm(IC<1>{});
    exch();
    {
      f32x4 st = *(const LDS_AS f32x4*)(lds + L_ST + row * 16);
      mean1 = st[0]; r1 = st[1];
      const float rs1 = st[2];
#pragma unroll
      for (int c = 0; c < 2; c++) {
        const char* cp = lds + L_CS + (32 * t + 16 * c + 8 * hh) * 4;
        f32x4 c0 = *(const LDS_AS f32x4*)cp, c1 = *(const LDS_AS f32x4*)(cp + 16);
        float v[8];
#pragma unroll
        for (int e = 0; e < 8; e++) v[e] = gelu_f(rs1 * (acc[0][8 * c + e] - mean1 * (e < 4 ? c0[e & 3] : c1[e & 3])));
        bst16(so640[c], WS_H1, u32x4{cvtpk(v[0], v[1]), cvtpk(v[2], v[3]), cvtpk(v[4], v[5]), cvtpk(v[6], v[7])});
      }
    }
    publish(IC<1>{});
    gemm(IC<2>{});
    exch();
    {
      float ps[2] = {0.f, 0.f}, pq[2] = {0.f, 0.f};
#pragma unroll
      for (int c = 0; c < 2; c++) {
#pragma unroll
        for (int d = 0; d < 4; d++) {
          const float v0 = (res(hr[c], 2 * d) - mean1) * r1 + acc[0][8 * c + 2 * d], v1 = (res(hr[c], 2 * d + 1) - mean1) * r1 + acc[0][8 * c + 2 * d + 1];
          x1r[c][d] = cvtpk(v0, v1);
          if (PSTAT) { ps[0] += v0; ps[1] += v1; pq[0] = fmaf(v0, v0, pq[0]); pq[1] = fmaf(v1, v1, pq[1]); }
        }
        bst16(so640[c], WS_X1, x1r[c]);
      }
      if (PSTAT) pubstat(1, ps[0] + ps[1], pq[0] + pq[1]);
    }
    publish(IC<2>{});
    gemm(IC<3>{});
    LBAR();
    {
      const int q0 = cb * 27 + 4 * w + hh;
      sfor<0, 2>([&](auto CI) {
        constexpr int c = decltype(CI)::value;
        const char* cp = lds + L_CS + 320 + (32 * w + 16 * c + 8 * hh) * 4;
        f32x4 c0 = *(const LDS_AS f32x4*)cp, c1 = *(const LDS_AS f32x4*)(cp + 16);
        sfor<0, 2>([&](auto PI) {
          constexpr int p = decltype(PI)::value, gi = 2 * c + p;
          constexpr int e0 = LS.step[99] + gi * CH_E3 < LS.epi[3] ? LS.step[99] + gi * CH_E3 : LS.epi[3];
          constexpr int e1 = gi == 3 ? LS.epi[3] : LS.step[99] + (gi + 1) * CH_E3 < LS.epi[3] ? LS.step[99] + (gi + 1) * CH_E3 : LS.epi[3];
          if constexpr (e1 > e0) { sfor<e0, e1>(wl); __builtin_amdgcn_sched_barrier(0); }
          f32x4 st = *(const LDS_AS f32x4*)(lds + L_ST + (32 * p + l31) * 16);
          float v[8];
#pragma unroll
          for (int e = 0; e < 8; e++) v[e] = gelu_f(st[1] * (acc[p][8 * c + e] - st[0] * (e < 4 ? c0[e & 3] : c1[e & 3])));
          bst16((unsigned)(rt * T1728) + frag_off(q0 + 2 * c, 32 * p + l31), WS_H2,
                u32x4{cvtpk(v[0], v[1]), cvtpk(v[2], v[3]), cvtpk(v[4], v[5]), cvtpk(v[6], v[7])});
          if constexpr (CH_E3 > 0) __builtin_amdgcn_sched_barrier(0);
        });
      });
    }
    publish(IC<3>{});
    gemm(IC<4>{});
    exch();
    {
      int ln = lane; asm volatile("" : "+v"(ln));
      bf16_t* op = outp + (size_t)(rt * 64 + 32 * kh + (ln & 31)) * D_ + (cb * 10 + 4 * t + (ln >> 5)) * 8;
#pragma unroll
      for (int c = 0; c < 2; c++) {
        u32x4 o;
#pragma unroll
        for (int d = 0; d < 4; d++)
          o[d] = cvtpk(res(hr[c], 2 * d) + res(x1r[c], 2 * d) + acc[0][8 * c + 2 * d],
                       res(hr[c], 2 * d + 1) + res(x1r[c], 2 * d + 1) + acc[0][8 * c + 2 * d + 1]);
        st16<OUT_ST>(op + 16 * c, o);
      }
    }
    CTS(19); CTS(23); CTS(27);
    if (w == 0) CTS_FLUSH(0);
    return;
  }

  const int a = w - 4, i = lane & 15, g = lane >> 4;
  const int row = 16 * a + i;
  bf16x8 G[59];
  const unsigned vG = cb * 102400 + a * 5120 + 4096 + lane * 16;
  const unsigned vG7 = cb * 276480 + a * 5120 + 4096 + lane * 16;
  const int b = a & 1;
  const unsigned vE = cb * 286720 + b * 14336 + (8 + a) * 1024 + lane * 16;
  const unsigned vO = cb * 286720 + (1 - b) * 14336 + (8 + a) * 1024 + lane * 16;
  const unsigned vS = cb * 286720 + b * 14336 + (12 + (a >> 1)) * 1024 + lane * 16;
  auto gl = [&](auto GI) {
    constexpr int n = decltype(GI)::value;
    if constexpr (GON == 0) { bf16x8 tmp_; asm volatile("" : "=v"(tmp_)); G[n] = tmp_; }
    else if constexpr (n < 5) G[n] = bload(vG, WS_WOT + n * 20480);
    else if constexpr (n < 10) G[n] = bload(vG, WS_W1T + (n - 5) * 20480);
    else if constexpr (n < 15) G[n] = bload(vG, WS_W2T + (n - 10) * 20480);
    else if constexpr (n < 45) {
      constexpr int c = (n - 15) / 6, k = (n - 15) % 6;
      if constexpr (k < 4) G[n] = bload((k & 1) ? vO : vE, WS_W3T + (4 * c + (k & ~1)) * 14336);
      else G[n] = bload(vS, WS_W3T + (4 * c + 2 * (k - 4)) * 14336);
    } else G[n] = bload(vG7, WS_W4T + (n - 45) * 20480);
  };
  const unsigned vT = rt * T640 + a * 1024 + lane * 16;
#define CH_GS {{9, 11, 12, 13, 15, 18}, {20, 22, 24, 26, 28, 30}, {32, 34, 35, 36, 37, 37}, {43, 47, 51, 55, 59, 59}}
  constexpr int GS[4][6] = CH_GS;
  constexpr int G2 = 7;
  u32x2 hr, x1r;
  const unsigned so640 = rt * T640 + frag_off(cb * 10 + 8 + (g >> 1), row) + (g & 1) * 8;
  float mean1 = 0.f, r1 = 0.f;
  const char* aR[4];
#pragma unroll
  for (int p = 0; p < 4; p++) aR[p] = lds + a * 4096 + (a ^ p) * 1024 + lane * 16;
  const char* aSt[3];
#pragma unroll
  for (int d = 1; d < 4; d++) aSt[d - 1] = lds + (a ^ d) * 4096 + a * 1024 + lane * 16;
  gu32* flags = (gu32*)(ws + WS_FL);
  auto poll = [&](int ph) {
    gu32* p = P2H && ph == 0 ? flags + 10240 + rt * ((32 >> P2FG) * P2FQ) + ((lane & 31) >> P2FG) * P2FQ + (lane & ((1 << P2FG) - 1))
                             : flags + (ph * 32 + rt) * 64 + (lane & 7);
#pragma unroll 1
    for (unsigned spins = 0; spins < (1u << 18); spins++) {
      unsigned v = __hip_atomic_load(p, __ATOMIC_RELAXED, __HIP_MEMORY_SCOPE_AGENT);
      if (__all(v == 1u)) break;
      __builtin_amdgcn_s_sleep(1);
    }
  };
  auto publish = [&](auto PH) {
    constexpr int ph = decltype(PH)::value;
    CTS(3 + 4 * ph);
    __builtin_amdgcn_sched_barrier(0);
    sfor<GS[ph][4], GS[ph][5]>(gl);
    if constexpr (!((DF >> ph) & 1)) {
      waitvmc<GON * (GS[ph][5] - GS[ph][4])>();
      SBAR();
    }
    CTS(4 + 4 * ph);
    if constexpr ((DF >> ph) & 1) {
      if constexpr (DF_DLY > 0) __builtin_amdgcn_s_sleep(DF_DLY);
    } else
    if constexpr (!HALO && FL1 && ph == 1) {
      const int r2 = tid == 256 ? rt : (cb & 2) ? rt - 1 : rt + 1;
      const int wd = tid == 256 ? cb : ((cb & 2) ? 12 : 8) + (cb & 1) + ((cb >> 2) << 1);
      if (tid < 258 && r2 >= 0 && r2 < 32) __hip_atomic_store(flags + (64 + r2) * 64 + wd, 1u, __ATOMIC_RELAXED, __HIP_MEMORY_SCOPE_AGENT);
    } else
    if (tid == 256) __hip_atomic_store(flags + ((ph + 1) * 32 + rt) * 64 + cb, POLLM ? (unsigned)(ph + 2) : 1u, __ATOMIC_RELAXED, __HIP_MEMORY_SCOPE_AGENT);
  };
  auto stat_acc = [&](const bf16x8& f, float& s, float& q) {
    u32x4 u = __builtin_bit_cast(u32x4, f);
#pragma unroll
    for (int d = 0; d < 4; d++) { s = dot2(u[d], 0x3F803F80u, s); q = dot2(u[d], u[d], q); }
  };
  auto stat_fin = [&](float s, float q) {
    s = xsum4(s); q = xsum4(q);
    float mean = s * (1.0f / D_);
    float var = fmaxf(q * (1.0f / D_) - mean * mean, 0.f);
    float ra = __builtin_amdgcn_rsqf(var + 1e-5f);
    float rb = ra * __builtin_amdgcn_rsqf(var * ra * ra + 1e-5f);
    if (g == 0) *(LDS_AS f32x4*)(lds + L_ST + row * 16) = f32x4{mean, ra, rb, 0.f};
    mean1 = mean; r1 = ra;
    return rb;
  };
  const unsigned vSP = (unsigned)(((rt * 24 + 3 * cb + 2) * 64 + row) * 8);
  auto pubstat = [&](int k, float v0, float v1, float v2, float v3) {
    const float s2 = xsum4((v0 + v1) + (v2 + v3)), q2 = xsum4(fmaf(v0, v0, v1 * v1) + fmaf(v2, v2, v3 * v3));
    if (g == 0) bst8(vSP, WS_ST + k * 393216, u32x2{__float_as_uint(s2), __float_as_uint(q2)});
  };
#define WTM 0
#define WTW 2
  auto wtouch = [&](auto K_) {
    constexpr int k = decltype(K_)::value;
    if constexpr ((WTM >> k) & 1) {
      if (a == WTW) {
        if constexpr (k < 3) bst8(so640, k == 0 ? WS_H : k == 1 ? WS_H1 : WS_X1, u32x2{0, 0});
        else bst8((unsigned)(rt * T1728) + frag_off(cb * 27 + 16 + 2 * a + (g >> 1), a * 16 + i) + (g & 1) * 8, WS_H2, u32x2{0, 0});
        if constexpr ((k == 0 || k == 2) && (WTM & 16)) { if (g == 0) bst8(vSP, WS_ST + (k >> 1) * 393216, u32x2{0, 0}); }
      }
    }
  };
  auto ldstat = [&](int k) {
    const unsigned v = (unsigned)(rt * 12288 + (lane >> 3) * 512 + a * 128 + (lane & 7) * 16);
#pragma unroll
    for (int j = 0; j < 3; j++) glds_sc1(ws + WS_ST + k * 393216 + j * 4096, v, L_SP + a * 3072 + j * 1024);
  };
  auto rdstat = [&]() {
    const char* p = lds + L_SP + a * 3072 + g * 256 + i * 8;
    f32x2 u[6];
#pragma unroll
    for (int j = 0; j < 6; j++) u[j] = *(const LDS_AS f32x2*)(p + (j >> 1) * 1024 + (j & 1) * 128);
    const f32x2 t = ((u[0] + u[1]) + (u[2] + u[3])) + (u[4] + u[5]);
    return stat_fin(t[0], t[1]);
  };
  float rs1 = 0.f;
  f32x4 ac[4];
  auto exch = [&]() {
#pragma unroll
    for (int p = 1; p < 4; p++) *(LDS_AS f32x4*)(lds + LX_A + (a ^ p) * 3072 + (p - 1) * 1024 + lane * 16) = ac[p];
    LBAR();
#pragma unroll
    for (int p = 1; p < 4; p++) ac[0] += *(const LDS_AS f32x4*)(lds + LX_A + a * 3072 + (p - 1) * 1024 + lane * 16);
  };
  auto tile4 = [&](auto PH, auto&& isA) {
    constexpr int ph = decltype(PH)::value;
    constexpr int G0 = ph == 0 ? G2 : GS[ph - 1][5], PC = 4;
    sfor<0, 4 * CH_E>(isA);
#pragma unroll
    for (int p = 0; p < 4; p++) ac[p] = f32x4{0, 0, 0, 0};
    bf16x8 f[2][4];
    sfor<0, 5 + AUXP>([&](auto CI) {
      constexpr int c = decltype(CI)::value;
      if constexpr (c < 5) {
        constexpr int GP = c == 0 ? G0 : GS[ph][c - 1];
        waitbarv<young5(CH_E, c, GON * (GS[ph][0] - G0), GON * (GS[ph][1] - GS[ph][0]), GON * (GS[ph][2] - GS[ph][1]), GON * (GS[ph][3] - GS[ph][2]))>();
#pragma unroll
        for (int p = 0; p < 4; p++) f[c & 1][p] = ldsfrag(aR[p] + c * 16384);
        if constexpr (c + CH_E < 5) { __builtin_amdgcn_sched_barrier(0); sfor<4 * (c + CH_E), 4 * (c + CH_E) + 4>(isA); __builtin_amdgcn_sched_barrier(0); }
        if constexpr (AUXP) __builtin_amdgcn_sched_barrier(0);
        sfor<GP, GS[ph][c]>(gl);
        if constexpr (AUXP) __builtin_amdgcn_sched_barrier(0);
      }
      if constexpr (c >= AUXP) {
#pragma unroll
        for (int p = 0; p < 4; p++) ac[p] = mfma(G[ph * 5 + c - AUXP], f[(c - AUXP) & 1][p], ac[p]);
      }
      if constexpr (ph == 1 && c == 0) rs1 = rdstat();
      __builtin_amdgcn_sched_barrier(0);
    });
  };
  wtouch(IC<0>{});
  p2([&]() { sfor<0, P2_B0>(gl); }, [&]() { sfor<P2_B0, P2_B1>(gl); });
  __builtin_amdgcn_sched_barrier(0);
  sfor<P2_B1, P2_B1 + P2_B2>(gl);
  waitvmc<GON * P2_B2>();
  SBAR();
  CTSP2(26);
  if (tid < 260) __hip_atomic_store(flags + 10240 + (4 * rg + tid - 256) * ((32 >> P2FG) * P2FQ) + (h2 >> P2FG) * P2FQ + (h2 & ((1 << P2FG) - 1)), 1u, __ATOMIC_RELAXED, __HIP_MEMORY_SCOPE_AGENT);
  if (tid >= 384 && tid < 458) {
    const char* p = lds + L_SP + (tid - 384) * 16;
    const f32x4 s = ((*(const LDS_AS f32x4*)p + *(const LDS_AS f32x4*)(p + 1184)) + (*(const LDS_AS f32x4*)(p + 2368) + *(const LDS_AS f32x4*)(p + 3552))) + *(const LDS_AS f32x4*)(p + 4736);
    *(LDS_AS f32x4*)(lds + L_CS + (tid - 384) * 16) = s;
  }
  __builtin_amdgcn_sched_barrier(0);
  sfor<P2_B1 + P2_B2, G2>(gl);
  hr = *(const u32x2*)(x + (size_t)(rt * 64 + row) * D_ + cb * 80 + 64 + 4 * g);
  __builtin_amdgcn_sched_barrier(0);
  if constexpr (POLL1 & 1) { if (a == 0) poll(0); SBAR(); } else poll(0);
  CTS(1);
  tile4(IC<0>{}, [&](auto KI) { constexpr int ks = decltype(KI)::value; glds_sc1(ws + WS_O + ks * 4096, vT, ks * 4096 + a * 1024); });
  CTS(2);
  exch();
  {
    const float v0 = bflo(hr[0]) + ac[0][0], v1 = bfhi(hr[0]) + ac[0][1], v2 = bflo(hr[1]) + ac[0][2], v3 = bfhi(hr[1]) + ac[0][3];
    hr[0] = cvtpk(v0, v1); hr[1] = cvtpk(v2, v3);
    bst8(so640, WS_H, hr);
    if (PSTAT) pubstat(0, v0, v1, v2, v3);
  }
  publish(IC<0>{});
  wtouch(IC<1>{});
  if constexpr (OUTPP == 0) {
    if constexpr (OUTPF >= 7) {
      if (a == 1 || a == 3 || (OUTPF == 8 && a == 2))
        st8<OUT_ST>(outp + (size_t)(rt * 64 + 16 * a + (lane & 15)) * D_ + cb * 80 + 64 + 4 * (lane >> 4), u32x2{0, 0});
    } else if (a == 1 || (OUTPF == 2 && a == 2))
      glds_pf((const char*)outp, (unsigned)((rt * 64 + OUT_ROW) * 1280 + OUT_COL), OUT_LDS);
  }
  if constexpr (DF & 1) {} else
  if constexpr (POLL1 & 2) { if (a == 0) poll(1); SBAR(); } else poll(1);
  CTS(5);
  if (PSTAT) ldstat(0);
  tile4(IC<1>{}, [&](auto KI) { constexpr int ks = decltype(KI)::value; glds_sc1(ws + WS_H + ks * 4096, vT, ks * 4096 + a * 1024); });
  CTS(6);
  {
    exch();
    f32x4 c4 = *(const LDS_AS f32x4*)(lds + L_CS + (64 + 4 * g) * 4);
    float v[4];
#pragma unroll
    for (int e = 0; e < 4; e++) v[e] = gelu_f(rs1 * (ac[0][e] - mean1 * c4[e]));
    bst8(so640, WS_H1, u32x2{cvtpk(v[0], v[1]), cvtpk(v[2], v[3])});
  }
  publish(IC<1>{});
  wtouch(IC<2>{});
  if constexpr (OUTPP == 1) {
    if constexpr (OUTPF >= 7) {
      if (a == 1 || a == 3 || (OUTPF == 8 && a == 2))
        st8<OUT_ST>(outp + (size_t)(rt * 64 + 16 * a + (lane & 15)) * D_ + cb * 80 + 64 + 4 * (lane >> 4), u32x2{0, 0});
    } else if (a == 1 || (OUTPF == 2 && a == 2))
      glds_pf((const char*)outp, (unsigned)((rt * 64 + OUT_ROW) * 1280 + OUT_COL), OUT_LDS);
  }
  {
    unsigned vSh[4];
#pragma unroll
    for (int k = 0; k < 4; k++) {
      const int sh = k == 0 ? 1 : k == 1 ? -1 : k == 2 ? 2 : -2;
      int r = row - sh + 64;
      vSh[k] = (unsigned)((rt + (r >> 6)) * T640 + ((r >> 4) & 3) * 1024 + (r & 15) * 16 + g * 256);
      asm volatile("" : "+v"(vSh[k]));
    }
    {
      const int j = lane - 20, up = a == 0;
      const int q = lane < 20 ? (up ? 0 : 20) + lane : (up ? 40 : 60) + (j < 40 ? j >> 1 : 0);
      const int hr_ = lane < 20 ? (up ? 63 : 0) : (up ? 62 : 0) + (j & 1);
      const unsigned vh = (unsigned)((rt + (up ? 0 : 2)) * T640) + frag_off(q, hr_);
      const unsigned vf = (unsigned)(WS_FL + ((64 + rt) * 64 + (lane & 7)) * 4);
      __builtin_amdgcn_sched_barrier(0);
      if constexpr (DF & 2) {} else {
      if (a == 0 || a == 3) {
#pragma unroll 1
        for (unsigned spins = 0; spins < (1u << 18); spins++) {
          unsigned f; u32x4 d;
          asm volatile("global_load_dword %0, %2, %4 sc1\n\tglobal_load_dwordx4 %1, %3, %5 sc1\n\ts_waitcnt vmcnt(0)"
                       : "=&v"(f), "=&v"(d) : "v"(vf), "v"(vh), "s"(ws), "s"(ws + WS_H1G) : "memory");
          if (__all(f == 1u && d[0] != ~0u && d[1] != ~0u && d[2] != ~0u && d[3] != ~0u)) break;
          __builtin_amdgcn_s_sleep(1);
        }
      } else if constexpr (!(POLL1 & 4)) poll(2);
      if constexpr (POLL1 & 4) SBAR();
      }
    }
    CTS(9);
    tile4(IC<2>{}, [&](auto KI) { constexpr int ks = decltype(KI)::value; glds_sc1(ws + WS_H1G + ks * 4096, vSh[ks / 5], ks * 4096 + a * 1024); });
  }
  CTS(10);
  exch();
  {
    const float v0 = (bflo(hr[0]) - mean1) * r1 + ac[0][0], v1 = (bfhi(hr[0]) - mean1) * r1 + ac[0][1];
    const float v2 = (bflo(hr[1]) - mean1) * r1 + ac[0][2], v3 = (bfhi(hr[1]) - mean1) * r1 + ac[0][3];
    x1r[0] = cvtpk(v0, v1); x1r[1] = cvtpk(v2, v3);
    bst8(so640, WS_X1, x1r);
    if (PSTAT) pubstat(1, v0, v1, v2, v3);
  }
  publish(IC<2>{});
  wtouch(IC<3>{});
  if constexpr (OUTPP == 2) {
    if constexpr (OUTPF >= 7) {
      if (a == 1 || a == 3 || (OUTPF == 8 && a == 2))
        st8<OUT_ST>(outp + (size_t)(rt * 64 + 16 * a + (lane & 15)) * D_ + cb * 80 + 64 + 4 * (lane >> 4), u32x2{0, 0});
    } else if (a == 1 || (OUTPF == 2 && a == 2))
      glds_pf((const char*)outp, (unsigned)((rt * 64 + OUT_ROW) * 1280 + OUT_COL), OUT_LDS);
  }
  if constexpr (DF & 4) {} else
  if constexpr (POLL1 & 8) { if (a == 0) poll(3); SBAR(); } else poll(3);
  CTS(13);
  if (PSTAT) ldstat(1);
  auto isA6 = [&](auto KI) { constexpr int ks = decltype(KI)::value; glds_sc1(ws + WS_X1 + ks * 4096, vT, ks * 4096 + a * 1024); };
  constexpr int E6 = AUXP6 ? CH_E6 : 5;
  sfor<0, 4 * E6>(isA6);
  {
    f32x4 ao[4], as[4];
#pragma unroll
    for (int u = 0; u < 4; u++) { ao[u] = f32x4{0, 0, 0, 0}; as[u] = f32x4{0, 0, 0, 0}; }
    const char *lE[4], *lO[4];
#pragma unroll
    for (int u = 0; u < 4; u++) {
      lE[u] = lds + b * 4096 + (u ^ a) * 1024 + lane * 16;
      lO[u] = lds + (1 - b) * 4096 + (u ^ a) * 1024 + lane * 16;
    }
    if constexpr (AUXP6) {
      constexpr int DD = AUXP6;
      bf16x8 f[DD + 1][4];
      auto rd = [&](auto QI) {
        constexpr int q = decltype(QI)::value, c = q >> 2, p = q & 3;
#pragma unroll
        for (int u = 0; u < 4; u++) f[q % (DD + 1)][u] = ldsfrag(((p & 1) ? lO[u] : lE[u]) + (4 * c + (p & ~1)) * 4096);
      };
      waitbarv<4 * (E6 - 1)>();
      CTSP6(21);
      sfor<0, DD>(rd);
      if constexpr (E6 < 5) { __builtin_amdgcn_sched_barrier(0); sfor<4 * E6, 4 * E6 + 4>(isA6); __builtin_amdgcn_sched_barrier(0); }
      {
        const float m1 = mean1, r1s = r1;
        rdstat();
        mean1 = m1; r1 = r1s;
      }
      __builtin_amdgcn_sched_barrier(0);
      sfor<0, 20>([&](auto QI) {
        constexpr int q = decltype(QI)::value, c = q >> 2, p = q & 3;
        if constexpr (q + DD < 20) {
          if constexpr (((q + DD) & 3) == 0) {
            constexpr int c1 = (q + DD) >> 2, GP = c1 == 1 ? GS[2][5] : GS[3][c1 - 2];
            sfor<GP, GS[3][c1 - 1]>(gl);
            waitbarv<young5(E6, c1, GON * (GS[3][0] - GS[2][5]), GON * (GS[3][1] - GS[3][0]), GON * (GS[3][2] - GS[3][1]), GON * (GS[3][3] - GS[3][2]))>();
            CTSP6(c1 == 1 ? 24 : c1 == 2 ? 25 : c1 == 3 ? 26 : 20);
          }
          rd(IC<q + DD>{});
          __builtin_amdgcn_sched_barrier(0);
          if constexpr (((q + DD) & 3) == 0 && ((q + DD) >> 2) + E6 < 5) {
            sfor<4 * (((q + DD) >> 2) + E6), 4 * (((q + DD) >> 2) + E6) + 4>(isA6);
            __builtin_amdgcn_sched_barrier(0);
          }
        }
#pragma unroll
        for (int u = 0; u < 4; u++) ao[u] = mfma(G[15 + 6 * c + p], f[q % (DD + 1)][u], ao[u]);
        if constexpr ((p & 1) == 0) {
#pragma unroll
          for (int u = 0; u < 4; u++) as[u] = mfma(G[15 + 6 * c + 4 + (p >> 1)], f[q % (DD + 1)][u], as[u]);
        }
        __builtin_amdgcn_sched_barrier(0);
      });
      sfor<GS[3][3], GS[3][4]>(gl);
      __builtin_amdgcn_sched_barrier(0);
    } else {
    sfor<0, 5>([&](auto CI) {
      constexpr int c = decltype(CI)::value;
      constexpr int GP = c == 0 ? GS[2][5] : GS[3][c - 1];
      waitbarv<4 * (4 - c) + GON * (GP - GS[2][5])>();
      if constexpr (c > 0) CTSP6(c == 1 ? 24 : c == 2 ? 25 : c == 3 ? 26 : 20);
      else CTSP6(21);
      sfor<0, 4>([&](auto PI) {
        constexpr int p = decltype(PI)::value;
        bf16x8 f[4];
#pragma unroll
        for (int u = 0; u < 4; u++) f[u] = ldsfrag(((p & 1) ? lO[u] : lE[u]) + (4 * c + (p & ~1)) * 4096);
#pragma unroll
        for (int u = 0; u < 4; u++) ao[u] = mfma(G[15 + 6 * c + p], f[u], ao[u]);
        if constexpr ((p & 1) == 0) {
#pragma unroll
          for (int u = 0; u < 4; u++) as[u] = mfma(G[15 + 6 * c + 4 + (p >> 1)], f[u], as[u]);
        }
      });
      if constexpr (c == 0) {
        const float m1 = mean1, r1s = r1;
        rdstat();
        mean1 = m1; r1 = r1s;
      }
      __builtin_amdgcn_sched_barrier(0);
      sfor<GP, GS[3][c]>(gl);
      __builtin_amdgcn_sched_barrier(0);
    });
    }
    CTS(14);
    *(LDS_AS f32x4*)(lds + LX_A + (a ^ 1) * 3072 + lane * 16) = as[1];
    *(LDS_AS f32x4*)(lds + LX_A + (a ^ 1) * 3072 + 1024 + lane * 16) = as[3];
    LBAR();
    as[0] += *(const LDS_AS f32x4*)(lds + LX_A + a * 3072 + lane * 16);
    as[2] += *(const LDS_AS f32x4*)(lds + LX_A + a * 3072 + 1024 + lane * 16);
    int ln = lane; asm volatile("" : "+v"(ln));
    const int i = ln & 15, g = ln >> 4;
    const f32x4 co = *(const LDS_AS f32x4*)(lds + L_CS + 320 + (128 + 16 * a + 4 * g) * 4);
    const int st = 12 + (a >> 1);
    const bool shv = !(st == 13 && g >= 2);
    const f32x4 cs = *(const LDS_AS f32x4*)(lds + L_CS + 320 + (shv ? (16 * st + 4 * g) * 4 : 0));
    const int qo = cb * 27 + 16 + 2 * a + (g >> 1), qs = cb * 27 + 16 + 2 * (st - 8) + (g >> 1);
#pragma unroll
    for (int u = 0; u < 4; u++) {
      const int r = (u ^ a) * 16 + i;
      f32x4 s4 = *(const LDS_AS f32x4*)(lds + L_ST + r * 16);
      float v[4];
#pragma unroll
      for (int e = 0; e < 4; e++) v[e] = gelu_f(s4[1] * (ao[u][e] - s4[0] * co[e]));
      bst8((unsigned)(rt * T1728) + frag_off(qo, r) + (g & 1) * 8, WS_H2, u32x2{cvtpk(v[0], v[1]), cvtpk(v[2], v[3])});
      if ((u & 1) == 0) {
#pragma unroll
        for (int e = 0; e < 4; e++) v[e] = gelu_f(s4[1] * (as[u][e] - s4[0] * cs[e]));
        if (shv) bst8((unsigned)(rt * T1728) + frag_off(qs, r) + (g & 1) * 8, WS_H2, u32x2{cvtpk(v[0], v[1]), cvtpk(v[2], v[3])});
      }
    }
  }
  publish(IC<3>{});
  if constexpr (OUTPP == 3) {
    if constexpr (OUTPF >= 7) {
      if (a == 1 || a == 3 || (OUTPF == 8 && a == 2))
        st8<OUT_ST>(outp + (size_t)(rt * 64 + 16 * a + (lane & 15)) * D_ + cb * 80 + 64 + 4 * (lane >> 4), u32x2{0, 0});
    } else if (a == 1 || (OUTPF == 2 && a == 2))
      glds_pf((const char*)outp, (unsigned)((rt * 64 + OUT_ROW) * 1280 + OUT_COL), OUT_LDS);
  }
  if constexpr (POLL1 & 16) { if (a == 0) poll(4); SBAR(); } else poll(4);
  CTS(17);
  {
    const unsigned vT7 = rt * T1728 + a * 1024 + lane * 16;
    auto issue = [&](auto CI) {
      constexpr int c = decltype(CI)::value;
      if constexpr (c < 14) {
        sfor<0, (c == 13 ? 2 : 4)>([&](auto KI) {
          constexpr int k = decltype(KI)::value;
          glds_sc1(ws + WS_H2 + (4 * c + k) * 4096, vT7, (c & 7) * 16384 + k * 4096 + a * 1024);
        });
      }
    };
    sfor<0, CH_E7>(issue);
#pragma unroll
    for (int p = 0; p < 4; p++) ac[p] = f32x4{0, 0, 0, 0};
    bf16x8 f[2][4];
    sfor<0, 14 + AUXP7>([&](auto CI) {
      constexpr int c = decltype(CI)::value;
      if constexpr (c < 14) {
        constexpr int last = hi7(c - 1);
        constexpr int young = (last - c) * 4 - (last == 13 && c < 13 ? 2 : 0);
        waitbarv<young>();
        if constexpr (AUXP7 == 0) sfor<last + 1, hi7(c) + 1>(issue);
        if (!P7X && (c < 13 || a < 2)) {
#pragma unroll
          for (int p = 0; p < 4; p++) f[c & 1][p] = ldsfrag(aR[p] + (c & 7) * 16384);
        }
        if constexpr (AUXP7 > 0) { __builtin_amdgcn_sched_barrier(0); sfor<last + 1, hi7(c) + 1>(issue); __builtin_amdgcn_sched_barrier(0); }
      }
      constexpr int m = AUXP7 == 2 ? c - 1 : c;
      if constexpr (m >= 0 && m < 14 && (AUXP7 != 1 || c < 14)) {
        if (!P7X && (m < 13 || a < 2)) {
#pragma unroll
          for (int p = 0; p < 4; p++) ac[p] = mfma(G[45 + m], f[m & 1][p], ac[p]);
        }
      }
      __builtin_amdgcn_sched_barrier(0);
    });
  }
  CTS(18);
  exch();
  {
    u32x2 o;
#pragma unroll
    for (int d = 0; d < 2; d++) o[d] = cvtpk(bflo(hr[d]) + bflo(x1r[d]) + ac[0][2 * d], bfhi(hr[d]) + bfhi(x1r[d]) + ac[0][2 * d + 1]);
    int ln = lane; asm volatile("" : "+v"(ln));
    st8<OUT_ST>(outp + (size_t)(rt * 64 + 16 * a + (ln & 15)) * D_ + cb * 80 + 64 + 4 * (ln >> 4), o);
  }
  CTS(19); CTS(23); CTS(27);
  if (w == 4) CTS_FLUSH(32);
}

extern "C" void kernel_launch(void* const* d_in, const int* in_sizes, int n_in, void* d_out, int out_size, void* d_ws,
                              size_t ws_size, hipStream_t stream) {
  const bf16_t* x = (const bf16_t*)d_in[0];
  const bf16_t* wqkv = (const bf16_t*)d_in[1];
  const bf16_t* wo = (const bf16_t*)d_in[2];
  const bf16_t* w1 = (const bf16_t*)d_in[4];
  const bf16_t* w2 = (const bf16_t*)d_in[5];
  const bf16_t* w3 = (const bf16_t*)d_in[6];
  const bf16_t* w4 = (const bf16_t*)d_in[7];
  char* ws = (char*)d_ws;
  bf16_t* out = (bf16_t*)d_out;
  (void)hipFuncSetAttribute((const void*)k_qkv, hipFuncAttributeMaxDynamicSharedMemorySize, K1_LDS);
  (void)hipFuncSetAttribute((const void*)k_chain, hipFuncAttributeMaxDynamicSharedMemorySize, CH_LDS);
  k_qkv<<<256, 512, K1_LDS, stream>>>(x, wqkv, wo, w1, w2, w3, w4, ws);
  k_chain<<<256, 512, CH_LDS, stream>>>(x, ws, out);
}
```
